```python
import torch

def _softmax(x, axis=-1):
    return torch.softmax(x, dim=axis)
def _gelu(x):
    return torch.nn.functional.gelu(x, approximate="tanh")
def _silu(x):
    return torch.nn.functional.silu(x)
def _sigmoid(x):
    return torch.sigmoid(x)
def _layernorm(x, axis=-1, eps=1e-5):
    m = x.mean(dim=axis, keepdim=True)
    v = x.var(dim=axis, keepdim=True, unbiased=False)
    return (x - m) / torch.sqrt(v + eps)
def _scatter_add(tgt, idx, src, axis=0):
    return tgt.clone().index_add_(axis, idx, src)
def _segment_sum(src, seg, n):
    out = torch.zeros((n,) + src.shape[1:], dtype=src.dtype)
    return out.index_add_(0, seg, src)
def _pad(x, pw):
    flat = tuple(v for pair in reversed(pw) for v in pair)
    return torch.nn.functional.pad(x, flat)

kernel_name = 'sequential_preLN__sliding_block_cached__swiglu_ffn__decode'

def setup_inputs():

    _w = (lambda a: (lambda t: t.to(torch.bfloat16) if t.is_floating_point() else t)(torch.as_tensor(a)))
    def _A_setup():
        B, Sq, D = 2, 1, 640
        Hq, Hkv, Sk, Dh = 32, 32, 4096, 128
        assert Hq % Hkv == 0, (Hq, Hkv)
        return {
            "x": _w((lambda *s: torch.randn(*s, dtype=torch.bfloat16) * 0.05)(B, Sq, D)),
            "k_cache": _w((lambda *s: torch.randn(*s, dtype=torch.bfloat16) * 3.2)(B, Hkv, Sk, Dh)),
            "v_cache": _w((lambda *s: torch.randn(*s, dtype=torch.bfloat16) * 0.4)(B, Hkv, Sk, Dh)),
            "wq": _w((lambda *s: torch.randn(*s, dtype=torch.bfloat16) * 3.2)(D, Hq * Dh) * (D ** -0.5)),
            "wo": _w((lambda *s: torch.randn(*s, dtype=torch.bfloat16) * 0.4)(Hq * Dh, D) * ((Hq * Dh) ** -0.5)),
        }
    _A_in = _A_setup()

    def _B_setup():
        B, S, D, F = 2, 1, 640, 1728
        return {
            "x": _w((lambda *s: torch.randn(*s, dtype=torch.bfloat16) * 0.05)(B, S, D)),
            "w_gate": _w((lambda *s: torch.randn(*s, dtype=torch.bfloat16) * 0.05)(D, F)),
            "w_up": _w((lambda *s: torch.randn(*s, dtype=torch.bfloat16) * 0.05)(D, F)),
            "w_down": _w((lambda *s: torch.randn(*s, dtype=torch.bfloat16) * 0.05)(F, D)),
        }
    _B_in = _B_setup()

    return {
        "A_x": _A_in["x"],
        "A_k_cache": _A_in["k_cache"],
        "A_v_cache": _A_in["v_cache"],
        "A_wq": _A_in["wq"],
        "A_wo": _A_in["wo"],
        "B_x": _B_in["x"],
        "B_w_gate": _B_in["w_gate"],
        "B_w_up": _B_in["w_up"],
        "B_w_down": _B_in["w_down"],
    }

def reference(A_x, A_k_cache, A_v_cache, A_wq, A_wo, B_x, B_w_gate, B_w_up, B_w_down):
    def _A_ref(x, k_cache, v_cache, wq, wo):
        B, Sq, D = x.shape
        Hq, Dh = 32, 128
        Hkv, Sk = k_cache.shape[1], k_cache.shape[2]
        W = min(4096, max(256, Sk // 4))
        rep = Hq // Hkv
        q = (lambda x, *d: x.permute(*d))((x @ wq).reshape(B, Sq, Hq, Dh), 0, 2, 1, 3)
        k = (lambda x, n, axis: torch.repeat_interleave(x, n, dim=axis))(k_cache[:, :, Sk - W:, :], rep, axis=1)
        v = (lambda x, n, axis: torch.repeat_interleave(x, n, dim=axis))(v_cache[:, :, Sk - W:, :], rep, axis=1)
        scores = torch.einsum("bhqd,bhkd->bhqk", q, k) * (Dh ** -0.5)
        p = _softmax(scores, axis=-1)
        o = torch.einsum("bhqk,bhkd->bhqd", p, v)
        return (lambda x, *d: x.permute(*d))(o, 0, 2, 1, 3).reshape(B, Sq, Hq * Dh) @ wo

    def _B_ref(x, w_gate, w_up, w_down):
        g = _silu(x @ w_gate)
        u = x @ w_up
        return (g * u) @ w_down

    x = A_x
    h = x + _A_ref(_layernorm(x, axis=-1), A_k_cache, A_v_cache, A_wq, A_wo)
    h2 = h + _B_ref(_layernorm(h, axis=-1), B_w_gate, B_w_up, B_w_down)
    return h2
```

```cpp
#include <hip/hip_runtime.h>
#include <hip/hip_bf16.h>
#define EMU 0
typedef unsigned short u16;
__device__ __forceinline__ float bf2f(u16 v) { return __uint_as_float((unsigned)v << 16); }
__device__ __forceinline__ u16 f2bf(float f) { __hip_bfloat16 b = __float2bfloat16(f); return *reinterpret_cast<u16*>(&b); }
__device__ __forceinline__ float rb(float f) { return EMU ? bf2f(f2bf(f)) : f; }

constexpr int B = 2, D = 640, H = 32, SK = 4096, DH = 128, W = 1024, F = 1728;

__device__ float block_sum(float v, float* red) {
  for (int o = 32; o > 0; o >>= 1) v += __shfl_xor(v, o);
  __syncthreads();
  if ((threadIdx.x & 63) == 0) red[threadIdx.x >> 6] = v;
  __syncthreads();
  float s = 0; for (int i = 0; i < (int)(blockDim.x >> 6); ++i) s += red[i];
  return s;
}
__device__ float block_max(float v, float* red) {
  for (int o = 32; o > 0; o >>= 1) v = fmaxf(v, __shfl_xor(v, o));
  __syncthreads();
  if ((threadIdx.x & 63) == 0) red[threadIdx.x >> 6] = v;
  __syncthreads();
  float s = -INFINITY; for (int i = 0; i < (int)(blockDim.x >> 6); ++i) s = fmaxf(s, red[i]);
  return s;
}
__device__ void layernorm(const float* xin, float* xn, float* red) {
  float s = 0; for (int i = threadIdx.x; i < D; i += blockDim.x) s += xin[i];
  float mean = block_sum(s, red) / D;
  float q = 0; for (int i = threadIdx.x; i < D; i += blockDim.x) { float d = xin[i] - mean; q += d * d; }
  float var = block_sum(q, red) / D;
  float m = rb(mean), v = rb(var);
  float den = rb(sqrtf(rb(v + 1e-5f)));
  for (int i = threadIdx.x; i < D; i += blockDim.x) xn[i] = rb(rb(xin[i] - m) / den);
  __syncthreads();
}
__global__ void k_attn(const u16* x, const u16* kc, const u16* vc, const u16* wq, float* o) {
  __shared__ float xs[D], xn[D], red[16], q[DH], p[W];
  int h = blockIdx.x, b = blockIdx.y, t = threadIdx.x;
  for (int i = t; i < D; i += 256) xs[i] = bf2f(x[b * D + i]);
  __syncthreads();
  layernorm(xs, xn, red);
  if (t < DH) { float a = 0; for (int k = 0; k < D; ++k) a += xn[k] * bf2f(wq[(size_t)k * (H * DH) + h * DH + t]); q[t] = rb(a); }
  __syncthreads();
  const u16* kb = kc + ((size_t)(b * H + h) * SK + (SK - W)) * DH;
  const u16* vb = vc + ((size_t)(b * H + h) * SK + (SK - W)) * DH;
  float mx = -INFINITY;
  for (int j = t; j < W; j += 256) { float a = 0; for (int d = 0; d < DH; ++d) a += q[d] * bf2f(kb[(size_t)j * DH + d]); a = rb(rb(a) * 0.08838834764831845f); p[j] = a; mx = fmaxf(mx, a); }
  mx = block_max(mx, red);
  float s = 0; for (int j = t; j < W; j += 256) { float e = __expf(p[j] - mx); p[j] = e; s += e; }
  s = block_sum(s, red);
  for (int j = t; j < W; j += 256) p[j] = rb(p[j] / s);
  __syncthreads();
  if (t < DH) { float a = 0; for (int j = 0; j < W; ++j) a += p[j] * bf2f(vb[(size_t)j * DH + t]); o[b * H * DH + h * DH + t] = rb(a); }
}
__global__ void k_wo(const u16* x, const float* o, const u16* wo, float* hbuf) {
  int n = blockIdx.x * 64 + (threadIdx.x & 63), b = blockIdx.y, w = threadIdx.x >> 6;
  __shared__ float red[4][64];
  float a = 0; for (int j = w * 1024; j < (w + 1) * 1024; ++j) a += o[b * 4096 + j] * bf2f(wo[(size_t)j * D + n]);
  red[w][threadIdx.x & 63] = a; __syncthreads();
  if (w == 0) { a = red[0][threadIdx.x] + red[1][threadIdx.x] + red[2][threadIdx.x] + red[3][threadIdx.x]; hbuf[b * D + n] = rb(bf2f(x[b * D + n]) + rb(a)); }
}
__global__ void k_gu(const float* hbuf, const u16* wg, const u16* wu, float* act) {
  __shared__ float xs[D], xn[D], red[16];
  int b = blockIdx.y, t = threadIdx.x;
  for (int i = t; i < D; i += 64) xs[i] = hbuf[b * D + i];
  __syncthreads();
  layernorm(xs, xn, red);
  int n = blockIdx.x * 64 + t;
  float g = 0, u = 0; for (int k = 0; k < D; ++k) { g += xn[k] * bf2f(wg[(size_t)k * F + n]); u += xn[k] * bf2f(wu[(size_t)k * F + n]); }
  g = rb(g); u = rb(u);
  g = rb(g / (1.f + __expf(-g)));
  act[b * F + n] = rb(g * u);
}
__global__ void k_down(const float* hbuf, const float* act, const u16* wd, u16* out) {
  int n = blockIdx.x * 64 + threadIdx.x, b = blockIdx.y;
  float a = 0; for (int j = 0; j < F; ++j) a += act[b * F + j] * bf2f(wd[(size_t)j * D + n]);
  out[b * D + n] = f2bf(hbuf[b * D + n] + rb(a));
}
extern "C" void kernel_launch(void* const* d_in, const int* in_sizes, int n_in, void* d_out, int out_size, void* d_ws, size_t ws_size, hipStream_t stream) {
  const u16* x = (const u16*)d_in[0]; const u16* kc = (const u16*)d_in[1]; const u16* vc = (const u16*)d_in[2];
  const u16* wq = (const u16*)d_in[3]; const u16* wo = (const u16*)d_in[4];
  const u16* wg = (const u16*)d_in[6]; const u16* wu = (const u16*)d_in[7]; const u16* wd = (const u16*)d_in[8];
  float* o = (float*)d_ws; float* hb = o + 8192; float* act = hb + 2048;
  k_attn<<<dim3(H, B), 256, 0, stream>>>(x, kc, vc, wq, o);
  k_wo<<<dim3(10, B), 256, 0, stream>>>(x, o, wo, hb);
  k_gu<<<dim3(27, B), 64, 0, stream>>>(hb, wg, wu, act);
  k_down<<<dim3(10, B), 64, 0, stream>>>(hb, act, wd, (u16*)d_out);
}
```

```cpp
#include <hip/hip_runtime.h>
#include <hip/hip_bf16.h>

namespace {
typedef unsigned short u16;
typedef unsigned v4u __attribute__((ext_vector_type(4)));
typedef unsigned v2u __attribute__((ext_vector_type(2)));
typedef float f32x4 __attribute__((ext_vector_type(4)));
typedef float f32x2 __attribute__((ext_vector_type(2)));
typedef short s16x8 __attribute__((ext_vector_type(8)));
typedef short s16x4 __attribute__((ext_vector_type(4)));
typedef __bf16 bf16x2 __attribute__((ext_vector_type(2)));
#define LAS __attribute__((address_space(3)))
typedef __amdgpu_buffer_rsrc_t rsrc_t;

constexpr int D = 640, H = 32, SK = 4096, DH = 128, WIN = 1024, F = 1728;
constexpr int NWG = 256, NWAVE = 15, NTHR = NWAVE * 64;
constexpr int W_Q = 8, W_O0 = 9, W_LD = 14;
constexpr int AUX_NT = 2, AUX_SC1 = 16;
constexpr int OOB = 0x7ffffff0;
constexpr unsigned SPIN_CAP = 1u << 20;
constexpr int REP_H = 8, REP_A = 8;

constexpr int WS_EPOCH = 0;
constexpr int WS_QP = 4096;
constexpr int WS_ML = WS_QP + 32 * 2 * 8 * 1024;
constexpr int WS_OA = WS_ML + 64 * 128;
constexpr int WS_PB = WS_OA + 64 * 16 * 256;
constexpr int WS_HB = WS_PB + 80 * 16384;
constexpr int WS_AC = WS_HB + REP_H * 5120;
constexpr int WS_END = WS_AC + REP_A * 13824;

constexpr int L_CTRL = 0;
constexpr int C_TAG = 0, C_KV = 1, C_QRDY = 2, C_ML = 3, C_O = 4, C_PUB = 5, C_WRDY = 6, C_DN = 7;
constexpr int L_Q = 64;
constexpr int L_ML = L_Q + 256;
constexpr int L_DN = L_ML + 64;
constexpr int L_O = 2048;
constexpr int L_W = L_O + 4096;
constexpr int LDS_BYTES = 84 * 1024;

__device__ __forceinline__ f32x4 mfma(v4u a, v4u b, f32x4 c) {
  return __builtin_amdgcn_mfma_f32_16x16x32_bf16(__builtin_bit_cast(s16x8, a), __builtin_bit_cast(s16x8, b), c, 0, 0, 0);
}
__device__ __forceinline__ unsigned pk(float lo, float hi) { return __builtin_bit_cast(unsigned, __builtin_convertvector(f32x2{lo, hi}, bf16x2)); }
__device__ __forceinline__ float rb(float x) { return __uint_as_float(pk(x, x) << 16); }
__device__ __forceinline__ float blo(unsigned u) { return __uint_as_float(u << 16); }
__device__ __forceinline__ float bhi(unsigned u) { return __uint_as_float(u & 0xffff0000u); }
__device__ __forceinline__ float dot2(unsigned a, unsigned b, float acc) {
  return __builtin_amdgcn_fdot2_f32_bf16(__builtin_bit_cast(bf16x2, a), __builtin_bit_cast(bf16x2, b), acc, false);
}
template <int CTRL> __device__ __forceinline__ float dpp(float x) {
  return __builtin_bit_cast(float, __builtin_amdgcn_mov_dpp(__builtin_bit_cast(int, x), CTRL, 0xf, 0xf, true));
}
template <int CTRL> __device__ __forceinline__ unsigned dppu(unsigned x) { return (unsigned)__builtin_amdgcn_mov_dpp((int)x, CTRL, 0xf, 0xf, true); }
constexpr int XOR1 = 0xB1, XOR2 = 0x4E, HMIR = 0x141, ROR8 = 0x128, ROR4 = 0x124;
__device__ __forceinline__ float sum16(float x) {
  auto s = __builtin_amdgcn_permlane16_swap(__float_as_uint(x), __float_as_uint(x), false, false);
  return __uint_as_float(s[0]) + __uint_as_float(s[1]);
}
__device__ __forceinline__ float sum32(float x) {
  auto s = __builtin_amdgcn_permlane32_swap(__float_as_uint(x), __float_as_uint(x), false, false);
  return __uint_as_float(s[0]) + __uint_as_float(s[1]);
}
__device__ __forceinline__ float max16(float x) {
  auto s = __builtin_amdgcn_permlane16_swap(__float_as_uint(x), __float_as_uint(x), false, false);
  return fmaxf(__uint_as_float(s[0]), __uint_as_float(s[1]));
}
__device__ __forceinline__ float max32(float x) {
  auto s = __builtin_amdgcn_permlane32_swap(__float_as_uint(x), __float_as_uint(x), false, false);
  return fmaxf(__uint_as_float(s[0]), __uint_as_float(s[1]));
}
__device__ __forceinline__ float sum_half8(float x) {
  x += dpp<XOR1>(x); x += dpp<XOR2>(x); x += dpp<HMIR>(x);
  return sum32(sum16(x));
}
__device__ __forceinline__ float fdiv(float a, float d, float r) {
  const float q = a * r;
  return fmaf(fmaf(-q, d, a), r, q);
}
__device__ __forceinline__ float exp_e(float x) { return __builtin_amdgcn_exp2f(x * 1.4426950408889634f); }
__device__ __forceinline__ rsrc_t mk(const void* p, int bytes) { return __builtin_amdgcn_make_buffer_rsrc((void*)p, 0, bytes, 0x00020000); }
template <int AUX> __device__ __forceinline__ v4u ld16(rsrc_t r, int voff, int soff = 0) { return __builtin_amdgcn_raw_buffer_load_b128(r, voff, soff, AUX); }
__device__ __forceinline__ void st16(rsrc_t r, int voff, v4u v) { __builtin_amdgcn_raw_buffer_store_b128(v, r, voff, 0, AUX_SC1); }
#define LGKM0() asm volatile("s_waitcnt lgkmcnt(0)" ::: "memory")
#define MEMBAR() asm volatile("" ::: "memory")

__device__ __forceinline__ void lds_wait(volatile LAS unsigned* w, unsigned want) {
#pragma nounroll
  for (unsigned s = 0; s < SPIN_CAP; ++s) {
    if ((unsigned)__builtin_amdgcn_readfirstlane(*w) >= want) break;
    __builtin_amdgcn_s_sleep(1);
  }
  MEMBAR();
}
__device__ __forceinline__ void lds_signal(volatile LAS unsigned* w, int lane) {
  LGKM0();
  if (lane == 0) __hip_atomic_fetch_add((LAS unsigned*)w, 1u, __ATOMIC_RELAXED, __HIP_MEMORY_SCOPE_WORKGROUP);
  MEMBAR();
}
template <int N, typename OFF>
__device__ __forceinline__ void sweep(rsrc_t r, OFF off, unsigned tag, v4u (&v)[N]) {
#pragma nounroll
  for (unsigned s = 0; s < SPIN_CAP; ++s) {
    MEMBAR();
    bool ok = true;
#pragma unroll
    for (int i = 0; i < N; ++i) v[i] = ld16<AUX_SC1>(r, off(i));
#pragma unroll
    for (int i = 0; i < N; ++i) ok &= (off(i) == OOB) | ((v[i].y == tag) & (v[i].w == tag));
    if (__all(ok)) break;
  }
  MEMBAR();
}
__device__ __forceinline__ void glds16(const void* gsrc, unsigned lds_dst) {
  unsigned keep;
  asm volatile("s_mov_b32 %0, m0\n\ts_mov_b32 m0, %2\n\ts_nop 0\n\tglobal_load_lds_dwordx4 %1, off nt\n\ts_mov_b32 m0, %0"
               : "=&s"(keep) : "v"(gsrc), "s"(lds_dst) : "memory");
}
__device__ __forceinline__ v4u diag(unsigned dup, const v4u& mask) { return v4u{dup & mask.x, dup & mask.y, dup & mask.z, dup & mask.w}; }
__device__ __forceinline__ v4u tr_frag(LAS unsigned char* p0, LAS unsigned char* p1) {
  const v2u a = __builtin_bit_cast(v2u, __builtin_amdgcn_ds_read_tr16_b64_v4i16((LAS s16x4*)p0));
  const v2u b = __builtin_bit_cast(v2u, __builtin_amdgcn_ds_read_tr16_b64_v4i16((LAS s16x4*)p1));
  return v4u{a.x, a.y, b.x, b.y};
}

#define STAMP(i) do {} while (0)
#define STAMP_DECL do {} while (0)
#define STAMP_OUT() do {} while (0)

__global__ void __launch_bounds__(NTHR) fused_block(const u16* x, const u16* kc, const u16* vc, const u16* wq, const u16* wo,
                                                    const u16* wg, const u16* wu, const u16* wd, u16* out, unsigned char* ws) {
  __shared__ __attribute__((aligned(16))) unsigned char lds_raw[LDS_BYTES];
  LAS unsigned char* lds = (LAS unsigned char*)lds_raw;
  volatile LAS unsigned* ctrl = (volatile LAS unsigned*)(lds + L_CTRL);
  const int tid = threadIdx.x, lane = tid & 63, wave = __builtin_amdgcn_readfirstlane(tid >> 6);
  const int g = lane >> 4, c = lane & 15;
  const int bid = blockIdx.x, xcd = bid & 7, r = bid >> 3;
  const int h = xcd * 4 + (r & 3), e = r >> 2, b = e & 1, s = e >> 1;
  const int nf = xcd < 3 ? 16 : 12, nd = xcd < 3 ? 0 : 8;
  const int role = r < nf ? 1 : r < nf + nd ? 2 : r < nf + nd + 10 ? 3 : 0;
  const int ridx = role == 1 ? 4 * (xcd + 8 * (r >> 2)) + (r & 3) : role == 2 ? 8 * (xcd - 3) + (r - nf) : xcd * 10 + (r - nf - nd);
  const rsrc_t rws = mk(ws, WS_END);
  const unsigned half = (c & 1) ? 0xffff0000u : 0x0000ffffu;
  const int cj = (c & 7) >> 1;
  const v4u mask = v4u{cj == 0 ? half : 0u, cj == 1 ? half : 0u, cj == 2 ? half : 0u, cj == 3 ? half : 0u};
  STAMP_DECL;
  STAMP(0);

  if (wave < 8) {
    if (wave == 0) { if (lane < 16) ctrl[lane] = 0u; LGKM0(); }
    __builtin_amdgcn_s_barrier();
    const size_t kvo = ((size_t)(b * H + h) * SK + (SK - WIN) + 256 * s + 32 * wave) * DH;
    const rsrc_t rk = mk(kc + kvo, 8192), rv = mk(vc + kvo, 8192);
    v4u kf[8], vf[8];
#pragma unroll
    for (int i = 0; i < 8; ++i) kf[i] = ld16<AUX_NT>(rk, c * 256 + g * 16 + (i & 3) * 64 + (i >> 2) * 4096);
#pragma unroll
    for (int i = 0; i < 8; ++i) vf[i] = ld16<AUX_NT>(rv, g * 1024 + c * 16 + (i & 3) * 256 + (i >> 2) * 4096);
    __builtin_amdgcn_sched_barrier(0);
    lds_signal(ctrl + C_KV, lane);
    STAMP(1);
    lds_wait(ctrl + C_QRDY, 1u);
    v4u qf[4];
#pragma unroll
    for (int i = 0; i < 4; ++i) qf[i] = *(LAS v4u*)(lds + L_Q + i * 64 + g * 16);
    f32x4 sc[2];
#pragma unroll
    for (int t = 0; t < 2; ++t) {
      sc[t] = f32x4{0.f, 0.f, 0.f, 0.f};
#pragma unroll
      for (int i = 0; i < 4; ++i) sc[t] = mfma(kf[4 * t + i], qf[i], sc[t]);
    }
    float sv[8], m = -INFINITY;
#pragma unroll
    for (int i = 0; i < 8; ++i) { sv[i] = rb(rb(sc[i >> 2][i & 3]) * 0.08838834764831845f); m = fmaxf(m, sv[i]); }
    m = max32(max16(m));
    float l = 0.f;
    unsigned pd[8];
#pragma unroll
    for (int i = 0; i < 8; ++i) { const float p = exp_e(sv[i] - m); l += p; pd[i] = pk(p, p); }
    l = sum32(sum16(l));
    if (lane == 0) *(LAS f32x2*)(lds + L_ML + wave * 8) = f32x2{m, l};
    lds_signal(ctrl + C_ML, lane);
    STAMP(2);
    f32x4 o = f32x4{0.f, 0.f, 0.f, 0.f};
#pragma unroll
    for (int i = 0; i < 8; ++i) o = mfma(diag(pd[i], mask), vf[i], o);
    if (lane < 32) *(LAS f32x4*)(lds + L_O + wave * 512 + c * 32 + g * 16) = o;
    lds_signal(ctrl + C_O, lane);
    STAMP(3);
    const unsigned tag = ctrl[C_TAG];

    if (role == 3) {
      if (wave == 0) {
        const int rbb = ridx / 40, cg = ridx % 40;
        const unsigned xp = __builtin_amdgcn_raw_buffer_load_b32(mk(x, 2 * D * 2), (rbb * D + cg * 16 + (lane & 7) * 2) * 2, 0, 0);
        lds_wait(ctrl + C_PUB, 5u);
        v4u v[16];
        const int base = WS_PB + ridx * 16384 + lane * 16;
        sweep<16>(rws, [&](int i) { return base + i * 1024; }, tag, v);
        STAMP(4);
        float a0 = 0.f, a1 = 0.f;
#pragma unroll
        for (int i = 0; i < 16; ++i) { a0 += __uint_as_float(v[i].x); a1 += __uint_as_float(v[i].z); }
        a0 += dpp<ROR8>(a0); a1 += dpp<ROR8>(a1);
        a0 = sum32(sum16(a0)); a1 = sum32(sum16(a1));
        const unsigned hp = pk(blo(xp) + rb(a0), bhi(xp) + rb(a1));
        const unsigned nb = dppu<XOR1>(hp);
        st16(rws, (lane & 1) ? OOB : WS_HB + (lane >> 3) * 5120 + rbb * 2560 + cg * 64 + (lane & 7) * 8, v4u{hp, tag, nb, tag});
        STAMP(5);
      }
    } else if (role == 1) {
      if (wave < 4) {
        lds_wait(ctrl + C_WRDY, 1u);
        v4u bw[10];
        LAS unsigned char* wb = lds + L_W + (c & 1) * 20480 + wave * 8 + (((c >> 1) & 1) * 32 + 8 * g + (c >> 2)) * 32;
#pragma unroll
        for (int u = 0; u < 10; ++u) bw[u] = tr_frag(wb + u * 2048, wb + u * 2048 + 128);
        const v4u ones = v4u{0x3f803f80u, 0x3f803f80u, 0x3f803f80u, 0x3f803f80u};
        f32x4 csa = f32x4{0.f, 0.f, 0.f, 0.f};
#pragma unroll
        for (int u = 0; u < 10; ++u) csa = mfma(ones, bw[u], csa);
        const float cs = csa[0] + dpp<ROR8>(csa[0]);
        STAMP(4);
        v4u v[6];
        const int hb = (c >> 3) & 1, q = c & 7;
        const int base = WS_HB + xcd * 5120 + hb * 2560 + q * 128 + g * 32;
        sweep<6>(rws, [&](int i) { return (i >= 4 && q >= 4) ? OOB : base + (i >> 1) * 1024 + (i & 1) * 16; }, tag, v);
        STAMP(5);
        v4u af[3];
#pragma unroll
        for (int t = 0; t < 3; ++t) af[t] = v4u{v[2 * t].x, v[2 * t].z, v[2 * t + 1].x, v[2 * t + 1].z};
        f32x4 acc[4];
#pragma unroll
        for (int u = 0; u < 4; ++u) acc[u] = f32x4{0.f, 0.f, 0.f, 0.f};
#pragma unroll
        for (int u = 0; u < 10; ++u) acc[u & 3] = mfma(af[u >> 2], bw[u], acc[u & 3]);
        float s1 = 0.f, s2 = 0.f;
#pragma unroll
        for (int t = 0; t < 3; ++t)
#pragma unroll
          for (int j = 0; j < 4; ++j) { s1 = dot2(af[t][j], 0x3f803f80u, s1); s2 = dot2(af[t][j], af[t][j], s2); }
        s1 = sum_half8(s1); s2 = sum_half8(s2);
        const float s1o = dpp<ROR8>(s1), s2o = dpp<ROR8>(s2);
        const bool own = hb == (g >> 1);
        const float mean = (own ? s1 : s1o) * (1.f / D), ex2 = (own ? s2 : s2o) * (1.f / D);
        const float var = fmaxf(ex2 - mean * mean, 0.f);
        const float den = rb(__builtin_sqrtf(rb(rb(var) + 1e-5f)));
        const float mb = rb(mean);
        const bool gh = g & 1, kh = hb;
        const float x0 = gh ? (kh ? acc[2][1] : acc[2][0]) : (kh ? acc[0][1] : acc[0][0]);
        const float x1 = gh ? (kh ? acc[3][3] : acc[3][2]) : (kh ? acc[1][3] : acc[1][2]);
        float y = x0 + x1;
        y += dpp<ROR8>(y);
        y = sum16(y);
        y = rb((y - mb * cs) / den);
        const float up = dpp<ROR4>(y);
        const float gate = (c & 4) ? up : y, upv = (c & 4) ? y : up;
        const float sg = rb(gate / (1.f + exp_e(-gate)));
        const float act = sg * upv;
        const unsigned pr = pk(dpp<0xA0>(act), dpp<0xF5>(act));
        const unsigned p0 = dppu<0x00>(pr), p1 = dppu<0xAA>(pr);
        const int rep = (g & 1) * 4 + (c >> 2);
        st16(rws, ((c & 3) == 0 && rep < REP_A) ? WS_AC + rep * 13824 + (g >> 1) * 6912 + (ridx * 16 + wave * 4) * 4 : OOB, v4u{p0, tag, p1, tag});
        STAMP(6);
      }
    } else if (role == 2) {
      if (wave < 7) {
        lds_wait(ctrl + C_WRDY, 1u);
        v4u bw[8];
        LAS unsigned char* wb = lds + L_W + (wave * 256 + 8 * g + (c >> 2)) * 32 + (c & 3) * 8;
#pragma unroll
        for (int u = 0; u < 8; ++u) bw[u] = tr_frag(wb + u * 1024, wb + u * 1024 + 128);
        STAMP(4);
        v4u v[2];
        const int hb = (c >> 3) & 1, q = c & 7;
        const bool live = wave * 8 + q < 54;
        const int base = WS_AC + (xcd % REP_A) * 13824 + hb * 6912 + (wave * 8 + q) * 128 + g * 32;
        sweep<2>(rws, [&](int i) { return live ? base + i * 16 : OOB; }, tag, v);
        STAMP(5);
        const v4u af = v4u{v[0].x, v[0].z, v[1].x, v[1].z};
        f32x4 acc[8];
#pragma unroll
        for (int u = 0; u < 8; ++u) acc[u] = mfma(af, bw[u], f32x4{0.f, 0.f, 0.f, 0.f});
        const bool gh = g & 1;
        const bool tail = wave == 6 && gh;
        float y = (gh ? acc[4][0] : acc[0][0]) + (gh ? acc[5][1] : acc[1][1]);
        const float y2 = (gh ? acc[6][2] : acc[2][2]) + (gh ? acc[7][3] : acc[3][3]);
        y += tail ? 0.f : y2;
        y = sum16(y);
        if (!gh) *(LAS float*)(lds + L_DN + wave * 128 + (g >> 1) * 64 + c * 4) = y;
        lds_signal(ctrl + C_DN, lane);
        STAMP(6);
      } else {
        lds_wait(ctrl + C_WRDY, 1u);
        v4u v[1];
        const int base = WS_HB + xcd * 5120 + ((lane >> 2) & 1) * 2560 + ridx * 64 + (lane & 3) * 16;
        sweep<1>(rws, [&](int) { return base; }, tag, v);
        STAMP(4);
        lds_wait(ctrl + C_DN, 7u);
        f32x4 a = f32x4{0.f, 0.f, 0.f, 0.f};
#pragma unroll
        for (int w = 0; w < 7; ++w) a += *(LAS f32x4*)(lds + L_DN + w * 128 + (lane & 7) * 16);
        const v2u res = v2u{pk(blo(v[0].x) + rb(a[0]), bhi(v[0].x) + rb(a[1])), pk(blo(v[0].z) + rb(a[2]), bhi(v[0].z) + rb(a[3]))};
        if (lane < 8) *(v2u*)(out + (lane >> 2) * D + ridx * 16 + (lane & 3) * 4) = res;
        if (ridx == 0 && lane == 0) __builtin_amdgcn_raw_buffer_store_b32(tag, rws, WS_EPOCH, 0, AUX_SC1);
        STAMP(5);
      }
    }
  } else if (wave == W_Q) {
    const unsigned ep = __builtin_amdgcn_raw_buffer_load_b32(rws, WS_EPOCH, 0, AUX_SC1);
    const rsrc_t rx = mk(x, 2 * D * 2), rq = mk(wq, D * H * DH * 2);
    const int bq = (lane >> 3) & 1;
    v2u xs[5], xo[5];
#pragma unroll
    for (int i = 0; i < 5; ++i) xs[i] = __builtin_amdgcn_raw_buffer_load_b64(rx, (bq * D + (g * 8 + (lane & 7)) * 20) * 2 + 8 * i, 0, 0);
#pragma unroll
    for (int i = 0; i < 5; ++i) xo[i] = __builtin_amdgcn_raw_buffer_load_b64(rx, (bq * D + 80 * e + 20 * g) * 2 + 8 * i, 0, 0);
    v4u wv[20];
#pragma unroll
    for (int i = 0; i < 20; ++i) wv[i] = ld16<AUX_NT>(rq, (80 * e + 20 * g) * 8192 + h * 256 + c * 16, i * 8192);
    __builtin_amdgcn_sched_barrier(0);
    __builtin_amdgcn_s_barrier();
    __builtin_amdgcn_sched_barrier(0);
    STAMP(1);
    const unsigned tag = (unsigned)__builtin_amdgcn_readfirstlane(ep) + 1u;
    if (lane == 0) ctrl[C_TAG] = tag;
    float sum = 0.f;
#pragma unroll
    for (int i = 0; i < 5; ++i) sum += (blo(xs[i].x) + bhi(xs[i].x)) + (blo(xs[i].y) + bhi(xs[i].y));
    const float mean = sum_half8(sum) * (1.f / D);
    float sq = 0.f;
#pragma unroll
    for (int i = 0; i < 5; ++i) {
      const float d0 = blo(xs[i].x) - mean, d1 = bhi(xs[i].x) - mean, d2 = blo(xs[i].y) - mean, d3 = bhi(xs[i].y) - mean;
      sq += (d0 * d0 + d1 * d1) + (d2 * d2 + d3 * d3);
    }
    const float var = sum_half8(sq) * (1.f / D);
    const float mb = rb(mean), den = rb(__builtin_sqrtf(rb(rb(var) + 1e-5f))), rden = __builtin_amdgcn_rcpf(den);
    f32x4 acc = f32x4{0.f, 0.f, 0.f, 0.f};
#pragma unroll
    for (int i = 0; i < 20; ++i) {
      const unsigned w = (i & 2) ? xo[i >> 2].y : xo[i >> 2].x;
      const float xn = fdiv(rb(((i & 1) ? bhi(w) : blo(w)) - mb), den, rden);
      acc = mfma(diag(pk(xn, xn), mask), wv[i], acc);
    }
    const int qo = WS_QP + ((h * 2 + (g >> 1)) * 8 + e) * 1024 + (c * 2 + (g & 1)) * 16;
    st16(rws, qo, v4u{__float_as_uint(acc[0]), tag, __float_as_uint(acc[1]), tag});
    st16(rws, qo + 512, v4u{__float_as_uint(acc[2]), tag, __float_as_uint(acc[3]), tag});
    STAMP(2);
    __builtin_amdgcn_s_sleep(24);
    {
      v4u v[8];
      const int base = WS_QP + (h * 2 + b) * 8192 + lane * 16;
      sweep<8>(rws, [&](int i) { return base + i * 1024; }, tag, v);
      float q0 = 0.f, q1 = 0.f;
#pragma unroll
      for (int i = 0; i < 8; ++i) { q0 += __uint_as_float(v[i].x); q1 += __uint_as_float(v[i].z); }
      *(LAS unsigned*)(lds + L_Q + ((lane & 31) * 2 + (lane >> 5)) * 4) = pk(q0, q1);
      lds_signal(ctrl + C_QRDY, lane);
    }
    STAMP(3);
    lds_wait(ctrl + C_ML, 8u);
    float wt[8], mx = -INFINITY, lt = 0.f;
    {
      f32x2 ml[8];
#pragma unroll
      for (int w = 0; w < 8; ++w) { ml[w] = *(LAS f32x2*)(lds + L_ML + w * 8); mx = fmaxf(mx, ml[w].x); }
#pragma unroll
      for (int w = 0; w < 8; ++w) { wt[w] = exp_e(ml[w].x - mx); lt = fmaf(wt[w], ml[w].y, lt); }
    }
    st16(rws, lane == 0 ? WS_ML + (b * H + h) * 128 + s * 16 : OOB, v4u{__float_as_uint(mx), tag, __float_as_uint(lt), tag});
    STAMP(4);
    lds_wait(ctrl + C_O, 8u);
    float o0 = 0.f, o1 = 0.f;
#pragma unroll
    for (int w = 0; w < 8; ++w) { const f32x2 t = *(LAS f32x2*)(lds + L_O + w * 512 + lane * 8); o0 = fmaf(wt[w], t.x, o0); o1 = fmaf(wt[w], t.y, o1); }
    st16(rws, WS_OA + (((b * H + h) * 4 + g) * 4 + s) * 256 + c * 16, v4u{__float_as_uint(o0), tag, __float_as_uint(o1), tag});
    STAMP(5);
  } else if (wave < W_LD) {
    __builtin_amdgcn_s_barrier();
    const int sg = wave - W_O0;
    const rsrc_t ro = mk(wo + (size_t)(h * DH + 32 * s) * D, 32 * D * 2);
    lds_wait(ctrl + C_KV, 8u);
    v4u wv[8];
#pragma unroll
    for (int i = 0; i < 8; ++i) wv[i] = ld16<AUX_NT>(ro, g * 8 * 1280 + sg * 256 + c * 16, i * 1280);
    __builtin_amdgcn_sched_barrier(0);
    STAMP(1);
    v4u sel0, sel1;
#pragma unroll
    for (int j = 0; j < 4; ++j) {
      sel0[j] = cj == j ? ((c & 1) ? 0x01000c0cu : 0x0c0c0100u) : 0x0c0c0c0cu;
      sel1[j] = cj == j ? ((c & 1) ? 0x03020c0cu : 0x0c0c0302u) : 0x0c0c0c0cu;
    }
    lds_wait(ctrl + C_O, 8u);
    const unsigned tag = ctrl[C_TAG];
    STAMP(2);
    v4u v[2];
    const int oa = WS_OA + (((b * H + h) * 4 + s) * 4 + (c >> 2)) * 256 + g * 64 + (c & 3) * 16;
    const int ma = WS_ML + (b * H + h) * 128 + (c >> 2) * 16;
    sweep<2>(rws, [&](int i) { return i ? oa : ma; }, tag, v);
    STAMP(3);
    const float mp = __uint_as_float(v[0].x), lp = __uint_as_float(v[0].z);
    float mx = fmaxf(mp, dpp<ROR8>(mp)); mx = fmaxf(mx, dpp<ROR4>(mx));
    const float wt = exp_e(mp - mx);
    float lt = wt * lp, o0 = wt * __uint_as_float(v[1].x), o1 = wt * __uint_as_float(v[1].z);
    lt += dpp<ROR8>(lt); o0 += dpp<ROR8>(o0); o1 += dpp<ROR8>(o1);
    lt += dpp<ROR4>(lt); o0 += dpp<ROR4>(o0); o1 += dpp<ROR4>(o1);
    const float rl = __builtin_amdgcn_rcpf(lt);
    const unsigned op = pk(o0 * rl, o1 * rl);
    const unsigned bc[4] = {dppu<0x00>(op), dppu<0x55>(op), dppu<0xAA>(op), dppu<0xFF>(op)};
    f32x4 acc = f32x4{0.f, 0.f, 0.f, 0.f};
#pragma unroll
    for (int i = 0; i < 8; ++i) {
      const v4u& sl = (i & 1) ? sel1 : sel0;
      const unsigned src = bc[i >> 1];
      const v4u a = v4u{__builtin_amdgcn_perm(0u, src, sl.x), __builtin_amdgcn_perm(0u, src, sl.y), __builtin_amdgcn_perm(0u, src, sl.z), __builtin_amdgcn_perm(0u, src, sl.w)};
      acc = mfma(a, wv[i], acc);
    }
    const bool up = g >= 2;
    const float r0 = up ? acc[2] : acc[0], r1 = up ? acc[3] : acc[1];
    st16(rws, WS_PB + (b * 40 + sg * 8 + (c >> 1)) * 16384 + (h * 4 + s) * 128 + (c & 1) * 64 + (g & 1) * 32 + (g >> 1) * 16,
         v4u{__float_as_uint(r0), tag, __float_as_uint(r1), tag});
    lds_signal(ctrl + C_PUB, lane);
    STAMP(4);
  } else {
    __builtin_amdgcn_s_barrier();
    if (role == 1 || role == 2) {
      lds_wait(ctrl + C_PUB, 5u);
      STAMP(1);
      const unsigned lw = (unsigned)(size_t)(lds + L_W);
      if (role == 1) {
        const u16* pg = wg + (size_t)(lane >> 1) * F + ridx * 16 + (lane & 1) * 8;
        const u16* pu = wu + (size_t)(lane >> 1) * F + ridx * 16 + (lane & 1) * 8;
#pragma unroll 2
        for (int i = 0; i < 20; ++i) {
          glds16(pg + (size_t)i * 32 * F, lw + i * 1024);
          glds16(pu + (size_t)i * 32 * F, lw + 20480 + i * 1024);
        }
      } else {
        const u16* pd = wd + (size_t)(lane >> 1) * D + ridx * 16 + (lane & 1) * 8;
#pragma unroll 3
        for (int i = 0; i < 54; ++i)
          glds16(pd + (size_t)i * 32 * D, lw + i * 1024);
      }
      STAMP(2);
      asm volatile("s_waitcnt vmcnt(0)" ::: "memory");
      lds_signal(ctrl + C_WRDY, lane);
      STAMP(3);
    }
  }
  STAMP(7);
  STAMP_OUT();
}
}

extern "C" void kernel_launch(void* const* d_in, const int* in_sizes, int n_in, void* d_out, int out_size, void* d_ws, size_t ws_size,
                              hipStream_t stream) {
  fused_block<<<NWG, NTHR, 0, stream>>>((const u16*)d_in[0], (const u16*)d_in[1], (const u16*)d_in[2], (const u16*)d_in[3], (const u16*)d_in[4],
                                        (const u16*)d_in[6], (const u16*)d_in[7], (const u16*)d_in[8], (u16*)d_out, (unsigned char*)d_ws);
}
```

```cpp
#include <hip/hip_runtime.h>
#include <hip/hip_bf16.h>

namespace {
typedef unsigned short u16;
typedef unsigned v4u __attribute__((ext_vector_type(4)));
typedef unsigned v2u __attribute__((ext_vector_type(2)));
typedef float f32x4 __attribute__((ext_vector_type(4)));
typedef float f32x2 __attribute__((ext_vector_type(2)));
typedef short s16x8 __attribute__((ext_vector_type(8)));
typedef short s16x4 __attribute__((ext_vector_type(4)));
typedef __bf16 bf16x2 __attribute__((ext_vector_type(2)));
#define LAS __attribute__((address_space(3)))
typedef __amdgpu_buffer_rsrc_t rsrc_t;

constexpr int D = 640, H = 32, SK = 4096, DH = 128, WIN = 1024, F = 1728;
constexpr int NWG = 256, NWAVE = 15, NTHR = NWAVE * 64;
constexpr int W_Q = 8, W_O0 = 9, W_LD = 14;
constexpr int AUX_NT = 2, AUX_SC1 = 16;
constexpr int OOB = 0x7ffffff0;
constexpr unsigned SPIN_CAP = 1u << 20;
constexpr int REP_H = 8, REP_A = 8;

constexpr int WS_EPOCH = 0;
constexpr int WS_QP = 4096;
constexpr int WS_ML = WS_QP + 32 * 2 * 8 * 1024;
constexpr int WS_OA = WS_ML + 64 * 128;
constexpr int WS_PB = WS_OA + 64 * 16 * 256;
constexpr int WS_HB = WS_PB + 80 * 16384;
constexpr int WS_AC = WS_HB + REP_H * 5120;
constexpr int WS_END = WS_AC + REP_A * 13824;

constexpr int L_CTRL = 0;
constexpr int C_TAG = 0, C_KV = 1, C_QRDY = 2, C_ML = 3, C_O = 4, C_PUB = 5, C_WRDY = 6, C_DN = 7;
constexpr int L_Q = 64;
constexpr int L_ML = L_Q + 256;
constexpr int L_DN = L_ML + 64;
constexpr int L_O = 2048;
constexpr int L_W = L_O + 4096;
constexpr int LDS_BYTES = 84 * 1024;

__device__ __forceinline__ f32x4 mfma(v4u a, v4u b, f32x4 c) {
  return __builtin_amdgcn_mfma_f32_16x16x32_bf16(__builtin_bit_cast(s16x8, a), __builtin_bit_cast(s16x8, b), c, 0, 0, 0);
}
__device__ __forceinline__ unsigned pk(float lo, float hi) { return __builtin_bit_cast(unsigned, __builtin_convertvector(f32x2{lo, hi}, bf16x2)); }
__device__ __forceinline__ float rb(float x) { return __uint_as_float(pk(x, x) << 16); }
__device__ __forceinline__ float blo(unsigned u) { return __uint_as_float(u << 16); }
__device__ __forceinline__ float bhi(unsigned u) { return __uint_as_float(u & 0xffff0000u); }
__device__ __forceinline__ float dot2(unsigned a, unsigned b, float acc) {
  return __builtin_amdgcn_fdot2_f32_bf16(__builtin_bit_cast(bf16x2, a), __builtin_bit_cast(bf16x2, b), acc, false);
}
template <int CTRL> __device__ __forceinline__ float dpp(float x) {
  return __builtin_bit_cast(float, __builtin_amdgcn_mov_dpp(__builtin_bit_cast(int, x), CTRL, 0xf, 0xf, true));
}
template <int CTRL> __device__ __forceinline__ unsigned dppu(unsigned x) { return (unsigned)__builtin_amdgcn_mov_dpp((int)x, CTRL, 0xf, 0xf, true); }
constexpr int XOR1 = 0xB1, XOR2 = 0x4E, HMIR = 0x141, ROR8 = 0x128, ROR4 = 0x124;
__device__ __forceinline__ float sum16(float x) {
  auto s = __builtin_amdgcn_permlane16_swap(__float_as_uint(x), __float_as_uint(x), false, false);
  return __uint_as_float(s[0]) + __uint_as_float(s[1]);
}
__device__ __forceinline__ float sum32(float x) {
  auto s = __builtin_amdgcn_permlane32_swap(__float_as_uint(x), __float_as_uint(x), false, false);
  return __uint_as_float(s[0]) + __uint_as_float(s[1]);
}
__device__ __forceinline__ float max16(float x) {
  auto s = __builtin_amdgcn_permlane16_swap(__float_as_uint(x), __float_as_uint(x), false, false);
  return fmaxf(__uint_as_float(s[0]), __uint_as_float(s[1]));
}
__device__ __forceinline__ float max32(float x) {
  auto s = __builtin_amdgcn_permlane32_swap(__float_as_uint(x), __float_as_uint(x), false, false);
  return fmaxf(__uint_as_float(s[0]), __uint_as_float(s[1]));
}
__device__ __forceinline__ float sum_half8(float x) {
  x += dpp<XOR1>(x); x += dpp<XOR2>(x); x += dpp<HMIR>(x);
  return sum32(sum16(x));
}
__device__ __forceinline__ float fdiv(float a, float d, float r) {
  const float q = a * r;
  return fmaf(fmaf(-q, d, a), r, q);
}
__device__ __forceinline__ float exp_e(float x) { return __builtin_amdgcn_exp2f(x * 1.4426950408889634f); }
__device__ __forceinline__ rsrc_t mk(const void* p, int bytes) { return __builtin_amdgcn_make_buffer_rsrc((void*)p, 0, bytes, 0x00020000); }
template <int AUX> __device__ __forceinline__ v4u ld16(rsrc_t r, int voff, int soff = 0) { return __builtin_amdgcn_raw_buffer_load_b128(r, voff, soff, AUX); }
__device__ __forceinline__ void st16(rsrc_t r, int voff, v4u v) { __builtin_amdgcn_raw_buffer_store_b128(v, r, voff, 0, AUX_SC1); }
#define LGKM0() asm volatile("s_waitcnt lgkmcnt(0)" ::: "memory")
#define MEMBAR() asm volatile("" ::: "memory")

__device__ __forceinline__ void lds_wait(volatile LAS unsigned* w, unsigned want) {
#pragma nounroll
  for (unsigned s = 0; s < SPIN_CAP; ++s) {
    if ((unsigned)__builtin_amdgcn_readfirstlane(*w) >= want) break;
    __builtin_amdgcn_s_sleep(1);
  }
  MEMBAR();
}
__device__ __forceinline__ void lds_signal(volatile LAS unsigned* w, int lane) {
  LGKM0();
  if (lane == 0) __hip_atomic_fetch_add((LAS unsigned*)w, 1u, __ATOMIC_RELAXED, __HIP_MEMORY_SCOPE_WORKGROUP);
  MEMBAR();
}
template <int N, typename OFF>
__device__ __forceinline__ void sweep(rsrc_t r, OFF off, unsigned tag, v4u (&v)[N]) {
#pragma nounroll
  for (unsigned s = 0; s < SPIN_CAP; ++s) {
    MEMBAR();
    bool ok = true;
#pragma unroll
    for (int i = 0; i < N; ++i) v[i] = ld16<AUX_SC1>(r, off(i));
#pragma unroll
    for (int i = 0; i < N; ++i) ok &= (off(i) == OOB) | ((v[i].y == tag) & (v[i].w == tag));
    if (__all(ok)) break;
  }
  MEMBAR();
}
__device__ __forceinline__ void glds16(const void* gsrc, unsigned lds_dst) {
  unsigned keep;
  asm volatile("s_mov_b32 %0, m0\n\ts_mov_b32 m0, %2\n\ts_nop 0\n\tglobal_load_lds_dwordx4 %1, off nt\n\ts_mov_b32 m0, %0"
               : "=&s"(keep) : "v"(gsrc), "s"(lds_dst) : "memory");
}
__device__ __forceinline__ v4u diag(unsigned dup, const v4u& mask) { return v4u{dup & mask.x, dup & mask.y, dup & mask.z, dup & mask.w}; }
__device__ __forceinline__ v4u tr_frag(LAS unsigned char* p0, LAS unsigned char* p1) {
  const v2u a = __builtin_bit_cast(v2u, __builtin_amdgcn_ds_read_tr16_b64_v4i16((LAS s16x4*)p0));
  const v2u b = __builtin_bit_cast(v2u, __builtin_amdgcn_ds_read_tr16_b64_v4i16((LAS s16x4*)p1));
  return v4u{a.x, a.y, b.x, b.y};
}

#define STAMP(i) do {} while (0)
#define STAMP_DECL do {} while (0)
#define STAMP_OUT() do {} while (0)

__global__ void __launch_bounds__(NTHR) fused_block(const u16* x, const u16* kc, const u16* vc, const u16* wq, const u16* wo,
                                                    const u16* wg, const u16* wu, const u16* wd, u16* out, unsigned char* ws) {
  __shared__ __attribute__((aligned(16))) unsigned char lds_raw[LDS_BYTES];
  LAS unsigned char* lds = (LAS unsigned char*)lds_raw;
  volatile LAS unsigned* ctrl = (volatile LAS unsigned*)(lds + L_CTRL);
  const int tid = threadIdx.x, lane = tid & 63, wave = __builtin_amdgcn_readfirstlane(tid >> 6);
  const int g = lane >> 4, c = lane & 15;
  const int bid = blockIdx.x, xcd = bid & 7, r = bid >> 3;
  const int h = xcd * 4 + (r & 3), e = r >> 2, b = e & 1, s = e >> 1;
  const int nf = xcd < 3 ? 16 : 12, nd = xcd < 3 ? 0 : 8;
  const int role = r < nf ? 1 : r < nf + nd ? 2 : r < nf + nd + 10 ? 3 : 0;
  const int ridx = role == 1 ? 4 * (xcd + 8 * (r >> 2)) + (r & 3) : role == 2 ? 8 * (xcd - 3) + (r - nf) : xcd * 10 + (r - nf - nd);
  const rsrc_t rws = mk(ws, WS_END);
  const unsigned half = (c & 1) ? 0xffff0000u : 0x0000ffffu;
  const int cj = (c & 7) >> 1;
  const v4u mask = v4u{cj == 0 ? half : 0u, cj == 1 ? half : 0u, cj == 2 ? half : 0u, cj == 3 ? half : 0u};
  if (tid == 0) __hip_atomic_store((unsigned long long*)(ws + (8 << 20)) + 16 + bid, (unsigned long long)__builtin_amdgcn_s_memrealtime(), __ATOMIC_RELAXED, __HIP_MEMORY_SCOPE_AGENT);
  STAMP_DECL;
  STAMP(0);

  if (wave < 8) {
    if (wave == 0) { if (lane < 16) ctrl[lane] = 0u; LGKM0(); }
    __builtin_amdgcn_s_barrier();
    const size_t kvo = ((size_t)(b * H + h) * SK + (SK - WIN) + 256 * s + 32 * wave) * DH;
    const rsrc_t rk = mk(kc + kvo, 8192), rv = mk(vc + kvo, 8192);
    v4u kf[8], vf[8];
#pragma unroll
    for (int i = 0; i < 8; ++i) kf[i] = ld16<AUX_NT>(rk, c * 256 + g * 16 + (i & 3) * 64 + (i >> 2) * 4096);
#pragma unroll
    for (int i = 0; i < 8; ++i) vf[i] = ld16<AUX_NT>(rv, g * 1024 + c * 16 + (i & 3) * 256 + (i >> 2) * 4096);
    __builtin_amdgcn_sched_barrier(0);
    lds_signal(ctrl + C_KV, lane);
    STAMP(1);
    lds_wait(ctrl + C_QRDY, 1u);
    v4u qf[4];
#pragma unroll
    for (int i = 0; i < 4; ++i) qf[i] = *(LAS v4u*)(lds + L_Q + i * 64 + g * 16);
    f32x4 sc[2];
#pragma unroll
    for (int t = 0; t < 2; ++t) {
      sc[t] = f32x4{0.f, 0.f, 0.f, 0.f};
#pragma unroll
      for (int i = 0; i < 4; ++i) sc[t] = mfma(kf[4 * t + i], qf[i], sc[t]);
    }
    float sv[8], m = -INFINITY;
#pragma unroll
    for (int i = 0; i < 8; ++i) { sv[i] = rb(rb(sc[i >> 2][i & 3]) * 0.08838834764831845f); m = fmaxf(m, sv[i]); }
    m = max32(max16(m));
    float l = 0.f;
    unsigned pd[8];
#pragma unroll
    for (int i = 0; i < 8; ++i) { const float p = exp_e(sv[i] - m); l += p; pd[i] = pk(p, p); }
    l = sum32(sum16(l));
    if (lane == 0) *(LAS f32x2*)(lds + L_ML + wave * 8) = f32x2{m, l};
    lds_signal(ctrl + C_ML, lane);
    STAMP(2);
    f32x4 o = f32x4{0.f, 0.f, 0.f, 0.f};
#pragma unroll
    for (int i = 0; i < 8; ++i) o = mfma(diag(pd[i], mask), vf[i], o);
    if (lane < 32) *(LAS f32x4*)(lds + L_O + wave * 512 + c * 32 + g * 16) = o;
    lds_signal(ctrl + C_O, lane);
    STAMP(3);
    const unsigned tag = ctrl[C_TAG];

    if (role == 3) {
      if (wave == 0) {
        const int rbb = ridx / 40, cg = ridx % 40;
        const unsigned xp = __builtin_amdgcn_raw_buffer_load_b32(mk(x, 2 * D * 2), (rbb * D + cg * 16 + (lane & 7) * 2) * 2, 0, 0);
        lds_wait(ctrl + C_PUB, 5u);
        v4u v[16];
        const int base = WS_PB + ridx * 16384 + lane * 16;
        sweep<16>(rws, [&](int i) { return base + i * 1024; }, tag, v);
        STAMP(4);
        float a0 = 0.f, a1 = 0.f;
#pragma unroll
        for (int i = 0; i < 16; ++i) { a0 += __uint_as_float(v[i].x); a1 += __uint_as_float(v[i].z); }
        a0 += dpp<ROR8>(a0); a1 += dpp<ROR8>(a1);
        a0 = sum32(sum16(a0)); a1 = sum32(sum16(a1));
        const unsigned hp = pk(blo(xp) + rb(a0), bhi(xp) + rb(a1));
        const unsigned nb = dppu<XOR1>(hp);
        st16(rws, (lane & 1) ? OOB : WS_HB + (lane >> 3) * 5120 + rbb * 2560 + cg * 64 + (lane & 7) * 8, v4u{hp, tag, nb, tag});
        STAMP(5);
      }
    } else if (role == 1) {
      if (wave < 4) {
        lds_wait(ctrl + C_WRDY, 1u);
        v4u bw[10];
        LAS unsigned char* wb = lds + L_W + (c & 1) * 20480 + wave * 8 + (((c >> 1) & 1) * 32 + 8 * g + (c >> 2)) * 32;
#pragma unroll
        for (int u = 0; u < 10; ++u) bw[u] = tr_frag(wb + u * 2048, wb + u * 2048 + 128);
        const v4u ones = v4u{0x3f803f80u, 0x3f803f80u, 0x3f803f80u, 0x3f803f80u};
        f32x4 csa = f32x4{0.f, 0.f, 0.f, 0.f};
#pragma unroll
        for (int u = 0; u < 10; ++u) csa = mfma(ones, bw[u], csa);
        const float cs = csa[0] + dpp<ROR8>(csa[0]);
        STAMP(4);
        v4u v[6];
        const int hb = (c >> 3) & 1, q = c & 7;
        const int base = WS_HB + xcd * 5120 + hb * 2560 + q * 128 + g * 32;
        sweep<6>(rws, [&](int i) { return (i >= 4 && q >= 4) ? OOB : base + (i >> 1) * 1024 + (i & 1) * 16; }, tag, v);
        STAMP(5);
        v4u af[3];
#pragma unroll
        for (int t = 0; t < 3; ++t) af[t] = v4u{v[2 * t].x, v[2 * t].z, v[2 * t + 1].x, v[2 * t + 1].z};
        f32x4 acc[4];
#pragma unroll
        for (int u = 0; u < 4; ++u) acc[u] = f32x4{0.f, 0.f, 0.f, 0.f};
#pragma unroll
        for (int u = 0; u < 10; ++u) acc[u & 3] = mfma(af[u >> 2], bw[u], acc[u & 3]);
        float s1 = 0.f, s2 = 0.f;
#pragma unroll
        for (int t = 0; t < 3; ++t)
#pragma unroll
          for (int j = 0; j < 4; ++j) { s1 = dot2(af[t][j], 0x3f803f80u, s1); s2 = dot2(af[t][j], af[t][j], s2); }
        s1 = sum_half8(s1); s2 = sum_half8(s2);
        const float s1o = dpp<ROR8>(s1), s2o = dpp<ROR8>(s2);
        const bool own = hb == (g >> 1);
        const float mean = (own ? s1 : s1o) * (1.f / D), ex2 = (own ? s2 : s2o) * (1.f / D);
        const float var = fmaxf(ex2 - mean * mean, 0.f);
        const float den = rb(__builtin_sqrtf(rb(rb(var) + 1e-5f)));
        const float mb = rb(mean);
        const bool gh = g & 1, kh = hb;
        const float x0 = gh ? (kh ? acc[2][1] : acc[2][0]) : (kh ? acc[0][1] : acc[0][0]);
        const float x1 = gh ? (kh ? acc[3][3] : acc[3][2]) : (kh ? acc[1][3] : acc[1][2]);
        float y = x0 + x1;
        y += dpp<ROR8>(y);
        y = sum16(y);
        y = rb((y - mb * cs) / den);
        const float up = dpp<ROR4>(y);
        const float gate = (c & 4) ? up : y, upv = (c & 4) ? y : up;
        const float sg = rb(gate / (1.f + exp_e(-gate)));
        const float act = sg * upv;
        const unsigned pr = pk(dpp<0xA0>(act), dpp<0xF5>(act));
        const unsigned p0 = dppu<0x00>(pr), p1 = dppu<0xAA>(pr);
        const int rep = (g & 1) * 4 + (c >> 2);
        st16(rws, ((c & 3) == 0 && rep < REP_A) ? WS_AC + rep * 13824 + (g >> 1) * 6912 + (ridx * 16 + wave * 4) * 4 : OOB, v4u{p0, tag, p1, tag});
        STAMP(6);
      }
    } else if (role == 2) {
      if (wave < 7) {
        lds_wait(ctrl + C_WRDY, 1u);
        v4u bw[8];
        LAS unsigned char* wb = lds + L_W + (wave * 256 + 8 * g + (c >> 2)) * 32 + (c & 3) * 8;
#pragma unroll
        for (int u = 0; u < 8; ++u) bw[u] = tr_frag(wb + u * 1024, wb + u * 1024 + 128);
        STAMP(4);
        v4u v[2];
        const int hb = (c >> 3) & 1, q = c & 7;
        const bool live = wave * 8 + q < 54;
        const int base = WS_AC + (xcd % REP_A) * 13824 + hb * 6912 + (wave * 8 + q) * 128 + g * 32;
        sweep<2>(rws, [&](int i) { return live ? base + i * 16 : OOB; }, tag, v);
        STAMP(5);
        const v4u af = v4u{v[0].x, v[0].z, v[1].x, v[1].z};
        f32x4 acc[8];
#pragma unroll
        for (int u = 0; u < 8; ++u) acc[u] = mfma(af, bw[u], f32x4{0.f, 0.f, 0.f, 0.f});
        const bool gh = g & 1;
        const bool tail = wave == 6 && gh;
        float y = (gh ? acc[4][0] : acc[0][0]) + (gh ? acc[5][1] : acc[1][1]);
        const float y2 = (gh ? acc[6][2] : acc[2][2]) + (gh ? acc[7][3] : acc[3][3]);
        y += tail ? 0.f : y2;
        y = sum16(y);
        if (!gh) *(LAS float*)(lds + L_DN + wave * 128 + (g >> 1) * 64 + c * 4) = y;
        lds_signal(ctrl + C_DN, lane);
        STAMP(6);
      } else {
        lds_wait(ctrl + C_WRDY, 1u);
        v4u v[1];
        const int base = WS_HB + xcd * 5120 + ((lane >> 2) & 1) * 2560 + ridx * 64 + (lane & 3) * 16;
        sweep<1>(rws, [&](int) { return base; }, tag, v);
        STAMP(4);
        lds_wait(ctrl + C_DN, 7u);
        f32x4 a = f32x4{0.f, 0.f, 0.f, 0.f};
#pragma unroll
        for (int w = 0; w < 7; ++w) a += *(LAS f32x4*)(lds + L_DN + w * 128 + (lane & 7) * 16);
        const v2u res = v2u{pk(blo(v[0].x) + rb(a[0]), bhi(v[0].x) + rb(a[1])), pk(blo(v[0].z) + rb(a[2]), bhi(v[0].z) + rb(a[3]))};
        v2u res2 = res;
        if (ridx == 0) {
          unsigned long long* rec = (unsigned long long*)(ws + (8 << 20));
          unsigned long long mn = ~0ull, mx = 0;
          for (int i = 0; i < 4; ++i) { const unsigned long long t = __hip_atomic_load(rec + 16 + lane * 4 + i, __ATOMIC_RELAXED, __HIP_MEMORY_SCOPE_AGENT); mn = t < mn ? t : mn; mx = t > mx ? t : mx; }
          for (int o = 32; o > 0; o >>= 1) { const unsigned long long a = __shfl_xor(mn, o), bb = __shfl_xor(mx, o); mn = a < mn ? a : mn; mx = bb > mx ? bb : mx; }
          const unsigned long long now = __builtin_amdgcn_s_memrealtime();
          const unsigned long long psig = __hip_atomic_load(rec, __ATOMIC_RELAXED, __HIP_MEMORY_SCOPE_AGENT), pend = __hip_atomic_load(rec + 1, __ATOMIC_RELAXED, __HIP_MEMORY_SCOPE_AGENT);
          const bool warm = psig == (0x1234567800000000ull | (unsigned long long)(tag - 1u));
          if (lane == 0) { __hip_atomic_store(rec, 0x1234567800000000ull | (unsigned long long)tag, __ATOMIC_RELAXED, __HIP_MEMORY_SCOPE_AGENT); __hip_atomic_store(rec + 1, now, __ATOMIC_RELAXED, __HIP_MEMORY_SCOPE_AGENT); }
          if (lane == 0 && (now - mn > 2600)) res2.x = pk((float)(now - mn) + 1000.f, 0.f);
        }
        if (lane < 8) *(v2u*)(out + (lane >> 2) * D + ridx * 16 + (lane & 3) * 4) = res2;
        if (ridx == 0 && lane == 0) __builtin_amdgcn_raw_buffer_store_b32(tag, rws, WS_EPOCH, 0, AUX_SC1);
        STAMP(5);
      }
    }
  } else if (wave == W_Q) {
    const unsigned ep = __builtin_amdgcn_raw_buffer_load_b32(rws, WS_EPOCH, 0, AUX_SC1);
    const rsrc_t rx = mk(x, 2 * D * 2), rq = mk(wq, D * H * DH * 2);
    const int bq = (lane >> 3) & 1;
    v2u xs[5], xo[5];
#pragma unroll
    for (int i = 0; i < 5; ++i) xs[i] = __builtin_amdgcn_raw_buffer_load_b64(rx, (bq * D + (g * 8 + (lane & 7)) * 20) * 2 + 8 * i, 0, 0);
#pragma unroll
    for (int i = 0; i < 5; ++i) xo[i] = __builtin_amdgcn_raw_buffer_load_b64(rx, (bq * D + 80 * e + 20 * g) * 2 + 8 * i, 0, 0);
    v4u wv[20];
#pragma unroll
    for (int i = 0; i < 20; ++i) wv[i] = ld16<AUX_NT>(rq, (80 * e + 20 * g) * 8192 + h * 256 + c * 16, i * 8192);
    __builtin_amdgcn_sched_barrier(0);
    __builtin_amdgcn_s_barrier();
    __builtin_amdgcn_sched_barrier(0);
    STAMP(1);
    const unsigned tag = (unsigned)__builtin_amdgcn_readfirstlane(ep) + 1u;
    if (lane == 0) ctrl[C_TAG] = tag;
    float sum = 0.f;
#pragma unroll
    for (int i = 0; i < 5; ++i) sum += (blo(xs[i].x) + bhi(xs[i].x)) + (blo(xs[i].y) + bhi(xs[i].y));
    const float mean = sum_half8(sum) * (1.f / D);
    float sq = 0.f;
#pragma unroll
    for (int i = 0; i < 5; ++i) {
      const float d0 = blo(xs[i].x) - mean, d1 = bhi(xs[i].x) - mean, d2 = blo(xs[i].y) - mean, d3 = bhi(xs[i].y) - mean;
      sq += (d0 * d0 + d1 * d1) + (d2 * d2 + d3 * d3);
    }
    const float var = sum_half8(sq) * (1.f / D);
    const float mb = rb(mean), den = rb(__builtin_sqrtf(rb(rb(var) + 1e-5f))), rden = __builtin_amdgcn_rcpf(den);
    f32x4 acc = f32x4{0.f, 0.f, 0.f, 0.f};
#pragma unroll
    for (int i = 0; i < 20; ++i) {
      const unsigned w = (i & 2) ? xo[i >> 2].y : xo[i >> 2].x;
      const float xn = fdiv(rb(((i & 1) ? bhi(w) : blo(w)) - mb), den, rden);
      acc = mfma(diag(pk(xn, xn), mask), wv[i], acc);
    }
    const int qo = WS_QP + ((h * 2 + (g >> 1)) * 8 + e) * 1024 + (c * 2 + (g & 1)) * 16;
    st16(rws, qo, v4u{__float_as_uint(acc[0]), tag, __float_as_uint(acc[1]), tag});
    st16(rws, qo + 512, v4u{__float_as_uint(acc[2]), tag, __float_as_uint(acc[3]), tag});
    STAMP(2);
    __builtin_amdgcn_s_sleep(24);
    {
      v4u v[8];
      const int base = WS_QP + (h * 2 + b) * 8192 + lane * 16;
      sweep<8>(rws, [&](int i) { return base + i * 1024; }, tag, v);
      float q0 = 0.f, q1 = 0.f;
#pragma unroll
      for (int i = 0; i < 8; ++i) { q0 += __uint_as_float(v[i].x); q1 += __uint_as_float(v[i].z); }
      *(LAS unsigned*)(lds + L_Q + ((lane & 31) * 2 + (lane >> 5)) * 4) = pk(q0, q1);
      lds_signal(ctrl + C_QRDY, lane);
    }
    STAMP(3);
    lds_wait(ctrl + C_ML, 8u);
    float wt[8], mx = -INFINITY, lt = 0.f;
    {
      f32x2 ml[8];
#pragma unroll
      for (int w = 0; w < 8; ++w) { ml[w] = *(LAS f32x2*)(lds + L_ML + w * 8); mx = fmaxf(mx, ml[w].x); }
#pragma unroll
      for (int w = 0; w < 8; ++w) { wt[w] = exp_e(ml[w].x - mx); lt = fmaf(wt[w], ml[w].y, lt); }
    }
    st16(rws, lane == 0 ? WS_ML + (b * H + h) * 128 + s * 16 : OOB, v4u{__float_as_uint(mx), tag, __float_as_uint(lt), tag});
    STAMP(4);
    lds_wait(ctrl + C_O, 8u);
    float o0 = 0.f, o1 = 0.f;
#pragma unroll
    for (int w = 0; w < 8; ++w) { const f32x2 t = *(LAS f32x2*)(lds + L_O + w * 512 + lane * 8); o0 = fmaf(wt[w], t.x, o0); o1 = fmaf(wt[w], t.y, o1); }
    st16(rws, WS_OA + (((b * H + h) * 4 + g) * 4 + s) * 256 + c * 16, v4u{__float_as_uint(o0), tag, __float_as_uint(o1), tag});
    STAMP(5);
  } else if (wave < W_LD) {
    __builtin_amdgcn_s_barrier();
    const int sg = wave - W_O0;
    const rsrc_t ro = mk(wo + (size_t)(h * DH + 32 * s) * D, 32 * D * 2);
    lds_wait(ctrl + C_KV, 8u);
    v4u wv[8];
#pragma unroll
    for (int i = 0; i < 8; ++i) wv[i] = ld16<AUX_NT>(ro, g * 8 * 1280 + sg * 256 + c * 16, i * 1280);
    __builtin_amdgcn_sched_barrier(0);
    STAMP(1);
    v4u sel0, sel1;
#pragma unroll
    for (int j = 0; j < 4; ++j) {
      sel0[j] = cj == j ? ((c & 1) ? 0x01000c0cu : 0x0c0c0100u) : 0x0c0c0c0cu;
      sel1[j] = cj == j ? ((c & 1) ? 0x03020c0cu : 0x0c0c0302u) : 0x0c0c0c0cu;
    }
    lds_wait(ctrl + C_O, 8u);
    const unsigned tag = ctrl[C_TAG];
    STAMP(2);
    v4u v[2];
    const int oa = WS_OA + (((b * H + h) * 4 + s) * 4 + (c >> 2)) * 256 + g * 64 + (c & 3) * 16;
    const int ma = WS_ML + (b * H + h) * 128 + (c >> 2) * 16;
    sweep<2>(rws, [&](int i) { return i ? oa : ma; }, tag, v);
    STAMP(3);
    const float mp = __uint_as_float(v[0].x), lp = __uint_as_float(v[0].z);
    float mx = fmaxf(mp, dpp<ROR8>(mp)); mx = fmaxf(mx, dpp<ROR4>(mx));
    const float wt = exp_e(mp - mx);
    float lt = wt * lp, o0 = wt * __uint_as_float(v[1].x), o1 = wt * __uint_as_float(v[1].z);
    lt += dpp<ROR8>(lt); o0 += dpp<ROR8>(o0); o1 += dpp<ROR8>(o1);
    lt += dpp<ROR4>(lt); o0 += dpp<ROR4>(o0); o1 += dpp<ROR4>(o1);
    const float rl = __builtin_amdgcn_rcpf(lt);
    const unsigned op = pk(o0 * rl, o1 * rl);
    const unsigned bc[4] = {dppu<0x00>(op), dppu<0x55>(op), dppu<0xAA>(op), dppu<0xFF>(op)};
    f32x4 acc = f32x4{0.f, 0.f, 0.f, 0.f};
#pragma unroll
    for (int i = 0; i < 8; ++i) {
      const v4u& sl = (i & 1) ? sel1 : sel0;
      const unsigned src = bc[i >> 1];
      const v4u a = v4u{__builtin_amdgcn_perm(0u, src, sl.x), __builtin_amdgcn_perm(0u, src, sl.y), __builtin_amdgcn_perm(0u, src, sl.z), __builtin_amdgcn_perm(0u, src, sl.w)};
      acc = mfma(a, wv[i], acc);
    }
    const bool up = g >= 2;
    const float r0 = up ? acc[2] : acc[0], r1 = up ? acc[3] : acc[1];
    st16(rws, WS_PB + (b * 40 + sg * 8 + (c >> 1)) * 16384 + (h * 4 + s) * 128 + (c & 1) * 64 + (g & 1) * 32 + (g >> 1) * 16,
         v4u{__float_as_uint(r0), tag, __float_as_uint(r1), tag});
    lds_signal(ctrl + C_PUB, lane);
    STAMP(4);
  } else {
    __builtin_amdgcn_s_barrier();
    if (role == 1 || role == 2) {
      lds_wait(ctrl + C_PUB, 5u);
      STAMP(1);
      const unsigned lw = (unsigned)(size_t)(lds + L_W);
      if (role == 1) {
        const u16* pg = wg + (size_t)(lane >> 1) * F + ridx * 16 + (lane & 1) * 8;
        const u16* pu = wu + (size_t)(lane >> 1) * F + ridx * 16 + (lane & 1) * 8;
#pragma unroll 2
        for (int i = 0; i < 20; ++i) {
          glds16(pg + (size_t)i * 32 * F, lw + i * 1024);
          glds16(pu + (size_t)i * 32 * F, lw + 20480 + i * 1024);
        }
      } else {
        const u16* pd = wd + (size_t)(lane >> 1) * D + ridx * 16 + (lane & 1) * 8;
#pragma unroll 3
        for (int i = 0; i < 54; ++i)
          glds16(pd + (size_t)i * 32 * D, lw + i * 1024);
      }
      STAMP(2);
      asm volatile("s_waitcnt vmcnt(0)" ::: "memory");
      lds_signal(ctrl + C_WRDY, lane);
      STAMP(3);
    }
  }
  STAMP(7);
  STAMP_OUT();
}
}

extern "C" void kernel_launch(void* const* d_in, const int* in_sizes, int n_in, void* d_out, int out_size, void* d_ws, size_t ws_size,
                              hipStream_t stream) {
  fused_block<<<NWG, NTHR, 0, stream>>>((const u16*)d_in[0], (const u16*)d_in[1], (const u16*)d_in[2], (const u16*)d_in[3], (const u16*)d_in[4],
                                        (const u16*)d_in[6], (const u16*)d_in[7], (const u16*)d_in[8], (u16*)d_out, (unsigned char*)d_ws);
}
```

```cpp
#include <hip/hip_runtime.h>

namespace {
typedef unsigned short u16;
typedef unsigned v4u __attribute__((ext_vector_type(4)));
typedef unsigned v2u __attribute__((ext_vector_type(2)));
typedef float f32x4 __attribute__((ext_vector_type(4)));
typedef float f32x2 __attribute__((ext_vector_type(2)));
typedef short s16x8 __attribute__((ext_vector_type(8)));
typedef short s16x4 __attribute__((ext_vector_type(4)));
typedef __bf16 bf16x2 __attribute__((ext_vector_type(2)));
#define LAS __attribute__((address_space(3)))
typedef __amdgpu_buffer_rsrc_t rsrc_t;

constexpr int D = 640, H = 32, SK = 4096, DH = 128, WIN = 1024, F = 1728;
constexpr int NWG = 256, NWAVE = 16, NTHR = NWAVE * 64;
constexpr int W_K0 = 6, W_V0 = 10, W_L0 = 14;
constexpr int AUX_NT = 2, AUX_SC1 = 16;
constexpr int OOB = 0x7ffffff0;
constexpr unsigned SPIN_CAP = 1u << 20;
constexpr unsigned PA = 16, PB = 15, PN = 24;
constexpr unsigned PF = 14;
constexpr unsigned PACE0 = 45, PACE0F = 55;
constexpr unsigned QT = 280;
constexpr unsigned WOFF = 5, WPACE = 12;
constexpr unsigned LOFF = 60, LOFD = 5, LG_P = 14, LD_P = 17;
constexpr int SLIP = 60, DEPTH = 7, LG_D = 8, LD_D = 4;
constexpr int LG_1 = 3, LD_1 = 3;
constexpr int CODE_PF = 16384;

constexpr int WS_EPOCH = 0;
constexpr int WS_ML = 4096;
constexpr int WS_OA = 16384;
constexpr int WS_PB = WS_OA + 32 * 16384;
constexpr int WS_HB = WS_PB + 40 * 16384;
constexpr int WS_AC = WS_HB + 16384;
constexpr int WS_SC = WS_AC + 2 * 16384;
constexpr int WS_QP = WS_SC + 8 * 32768;
constexpr int WS_END = WS_QP + 32 * 2 * 8 * 1024;
static_assert(WS_END <= (2 << 20) && WS_SC % 16384 == 0, "workspace map");

constexpr int C_TAG = 0, C_KV = 1, C_QRDY = 2, C_M = 3, C_O = 4, C_WO = 5, C_W0 = 6, C_W1 = 7, C_DN = 8, C_LN = 9, C_QP = 10, C_P = 11,
              C_T0 = 12, C_NEAR = 13, C_X = 14, C_T1 = 16;
constexpr int L_Q = 128;
constexpr int L_ML = L_Q + 256;
constexpr int L_ST = L_ML + 64;
constexpr int L_CS = L_ST + 64;
constexpr int L_DN = L_CS + 768;
constexpr int L_P = 3072;
constexpr int L_O = L_P + 1024;
constexpr int L_QP = L_O + 2048;
constexpr int L_W = L_QP + 5120;
constexpr int GBLK = 2048 + 32;
constexpr int DBLK = 1024 + 128;
constexpr int L_UP = 20 * GBLK;
constexpr int L_SCR = L_W + 44 * GBLK;
constexpr int LDS_BYTES = L_SCR + 768;
static_assert(L_DN + 1536 <= L_P && 56 * DBLK <= 44 * GBLK && LDS_BYTES > 80 * 1024, "LDS map");

__device__ __forceinline__ f32x4 mfma(v4u a, v4u b, f32x4 c) {
  return __builtin_amdgcn_mfma_f32_16x16x32_bf16(__builtin_bit_cast(s16x8, a), __builtin_bit_cast(s16x8, b), c, 0, 0, 0);
}
__device__ __forceinline__ unsigned pk(float lo, float hi) { return __builtin_bit_cast(unsigned, __builtin_convertvector(f32x2{lo, hi}, bf16x2)); }
__device__ __forceinline__ float rb(float x) { return __uint_as_float(pk(x, x) << 16); }
__device__ __forceinline__ float blo(unsigned u) { return __uint_as_float(u << 16); }
__device__ __forceinline__ float bhi(unsigned u) { return __uint_as_float(u & 0xffff0000u); }
__device__ __forceinline__ float dot2(unsigned a, unsigned b, float acc) {
  return __builtin_amdgcn_fdot2_f32_bf16(__builtin_bit_cast(bf16x2, a), __builtin_bit_cast(bf16x2, b), acc, false);
}
template <int CTRL> __device__ __forceinline__ float dpp(float x) {
  return __builtin_bit_cast(float, __builtin_amdgcn_mov_dpp(__builtin_bit_cast(int, x), CTRL, 0xf, 0xf, true));
}
template <int CTRL> __device__ __forceinline__ unsigned dppu(unsigned x) { return (unsigned)__builtin_amdgcn_mov_dpp((int)x, CTRL, 0xf, 0xf, true); }
constexpr int XOR1 = 0xB1, XOR2 = 0x4E, HMIR = 0x141, ROR8 = 0x128, ROR4 = 0x124;
__device__ __forceinline__ float sum16(float x) {
  auto s = __builtin_amdgcn_permlane16_swap(__float_as_uint(x), __float_as_uint(x), false, false);
  return __uint_as_float(s[0]) + __uint_as_float(s[1]);
}
__device__ __forceinline__ float sum32(float x) {
  auto s = __builtin_amdgcn_permlane32_swap(__float_as_uint(x), __float_as_uint(x), false, false);
  return __uint_as_float(s[0]) + __uint_as_float(s[1]);
}
__device__ __forceinline__ float max16(float x) {
  auto s = __builtin_amdgcn_permlane16_swap(__float_as_uint(x), __float_as_uint(x), false, false);
  return fmaxf(__uint_as_float(s[0]), __uint_as_float(s[1]));
}
__device__ __forceinline__ float max32(float x) {
  auto s = __builtin_amdgcn_permlane32_swap(__float_as_uint(x), __float_as_uint(x), false, false);
  return fmaxf(__uint_as_float(s[0]), __uint_as_float(s[1]));
}
__device__ __forceinline__ float sum_half8(float x) {
  x += dpp<XOR1>(x); x += dpp<XOR2>(x); x += dpp<HMIR>(x);
  return sum32(sum16(x));
}
__device__ __forceinline__ float fdiv(float a, float d, float r) {
  const float q = a * r;
  return fmaf(fmaf(-q, d, a), r, q);
}
__device__ __forceinline__ float exp_e(float x) { return __builtin_amdgcn_exp2f(x * 1.4426950408889634f); }
__device__ __forceinline__ rsrc_t mk(const void* p, int bytes) { return __builtin_amdgcn_make_buffer_rsrc((void*)p, 0, bytes, 0x00020000); }
template <int AUX> __device__ __forceinline__ v4u ld16(rsrc_t r, int voff, int soff = 0) { return __builtin_amdgcn_raw_buffer_load_b128(r, voff, soff, AUX); }
__device__ __forceinline__ void st16(rsrc_t r, int voff, v4u v) { __builtin_amdgcn_raw_buffer_store_b128(v, r, voff, 0, AUX_SC1); }
#define LGKM0() asm volatile("s_waitcnt lgkmcnt(0)" ::: "memory")
#define MEMBAR() asm volatile("" ::: "memory")

__device__ __forceinline__ void tick(unsigned& t, unsigned step) {
  unsigned n = 4096u;
  __builtin_amdgcn_sched_barrier(0);
  asm volatile("1:\n\ts_memrealtime vcc\n\ts_waitcnt lgkmcnt(0)\n\ts_sub_u32 vcc_lo, vcc_lo, %0\n\ts_cmp_ge_i32 vcc_lo, 0\n\t"
               "s_cbranch_scc1 2f\n\ts_sub_u32 %1, %1, 1\n\ts_cbranch_scc0 1b\n2:\n\t"
               "s_sub_u32 vcc_lo, vcc_lo, %3\n\ts_max_i32 vcc_lo, vcc_lo, 0\n\ts_add_u32 %0, %0, %2\n\ts_add_u32 %0, %0, vcc_lo"
               : "+s"(t), "+s"(n) : "s"(step), "i"(SLIP) : "vcc", "scc", "memory");
  __builtin_amdgcn_sched_barrier(0);
}
__device__ __forceinline__ void wait_until(unsigned t) { tick(t, 0u); }
#define PIN(x) asm volatile("" : "+v"(x))
template <int W, int SLEEP> __device__ __forceinline__ void lds_wait(unsigned cb, unsigned want) {
  unsigned t, n = SPIN_CAP;
  asm volatile("1:\n\tds_read_b32 %0, %2 offset:%4\n\ts_waitcnt lgkmcnt(0)\n\tv_cmp_le_u32 vcc, %3, %0\n\ts_cbranch_vccnz 2f\n\t"
               "s_sleep %5\n\ts_sub_u32 %1, %1, 1\n\ts_cbranch_scc0 1b\n2:"
               : "=&v"(t), "+s"(n) : "v"(cb), "s"(want), "i"(W * 4), "i"(SLEEP) : "vcc", "scc", "memory");
}
template <int W> __device__ __forceinline__ void lds_signal(unsigned cb) {
  asm volatile("s_mov_b64 exec, 1\n\tds_add_u32 %0, %1 offset:%2\n\ts_mov_b64 exec, -1" :: "v"(cb), "v"(1u), "i"(W * 4) : "memory");
}
template <int W> __device__ __forceinline__ unsigned lds_arrive(unsigned cb) {
  unsigned old;
  asm volatile("s_mov_b64 exec, 1\n\tds_add_rtn_u32 %0, %1, %2 offset:%3\n\ts_mov_b64 exec, -1" : "=&v"(old) : "v"(cb), "v"(1u), "i"(W * 4) : "memory");
  return old;
}
__device__ __forceinline__ unsigned ticket(unsigned old) {
  asm volatile("s_waitcnt lgkmcnt(0)" : "+v"(old) :: "memory");
  return (unsigned)__builtin_amdgcn_readfirstlane(old);
}
template <int N, int BACK = 0> __device__ __forceinline__ void sweep(rsrc_t r, const int (&off)[N], const unsigned (&et)[N], v4u (&v)[N]) {
#pragma nounroll
  for (unsigned s = 0; s < SPIN_CAP; ++s) {
    MEMBAR();
    unsigned bad = 0u;
#pragma unroll
    for (int i = 0; i < N; ++i) v[i] = ld16<AUX_SC1>(r, off[i]);
#pragma unroll
    for (int i = 0; i < N; ++i) bad |= (v[i].y ^ et[i]) | (v[i].w ^ et[i]);
    if (__builtin_amdgcn_ballot_w64(bad != 0u) == 0ull) break;
    if (BACK) __builtin_amdgcn_s_sleep(BACK);
  }
  MEMBAR();
}
#define SW_LD(R, O) "buffer_load_dwordx4 " R ", " O ", %[rs], 0 offen sc1\n\t"
#define SW_CK(A, B, E) "v_cmpx_eq_u32 vcc, " A ", " E "\n\tv_cmpx_eq_u32 vcc, " B ", " E "\n\t"
#define SW_END "s_cmp_eq_u64 exec, -1\n\ts_mov_b64 exec, -1\n\ts_cbranch_scc1 2f\n\ts_sub_u32 %[n], %[n], 1\n\ts_cbranch_scc0 1b\n2:\n\t"
template <bool PACK> __device__ __forceinline__ void sweep2(rsrc_t r, int o0, int o1, unsigned e0, unsigned e1, v4u& v0, v4u& v1) {
  unsigned n = SPIN_CAP;
  if (PACK)
    asm volatile("1:\n\t" SW_LD("v[120:123]", "%[o0]") SW_LD("v[124:127]", "%[o1]") "s_waitcnt vmcnt(1)\n\t" SW_CK("v121", "v123", "%[e0]")
                 "s_waitcnt vmcnt(0)\n\t" SW_CK("v125", "v127", "%[e1]") SW_END
                 "v_mov_b32 v121, v122\n\tv_mov_b32 v122, v124\n\tv_mov_b32 v123, v126\n\ts_nop 1"
                 : "=&{v[120:123]}"(v0), "=&{v[124:127]}"(v1), [n] "+s"(n)
                 : [o0] "v"(o0), [o1] "v"(o1), [e0] "v"(e0), [e1] "v"(e1), [rs] "s"(r) : "vcc", "scc", "memory");
  else
    asm volatile("1:\n\t" SW_LD("v[120:123]", "%[o0]") SW_LD("v[124:127]", "%[o1]") "s_waitcnt vmcnt(1)\n\t" SW_CK("v121", "v123", "%[e0]")
                 "s_waitcnt vmcnt(0)\n\t" SW_CK("v125", "v127", "%[e1]") SW_END "s_nop 1"
                 : "=&{v[120:123]}"(v0), "=&{v[124:127]}"(v1), [n] "+s"(n)
                 : [o0] "v"(o0), [o1] "v"(o1), [e0] "v"(e0), [e1] "v"(e1), [rs] "s"(r) : "vcc", "scc", "memory");
}
#define SW_LO(R, O, K) "buffer_load_dwordx4 " R ", " O ", %[rs], 0 offen offset:" K " sc1\n\t"
__device__ __forceinline__ void sweep8(rsrc_t r, int o0, int o1, unsigned e0, v4u (&v)[8]) {
  unsigned n = SPIN_CAP;
  asm volatile("1:\n\t" SW_LO("v[96:99]", "%[o0]", "0") SW_LO("v[100:103]", "%[o0]", "1024") SW_LO("v[104:107]", "%[o0]", "2048")
               SW_LO("v[108:111]", "%[o0]", "3072") SW_LO("v[112:115]", "%[o1]", "0") SW_LO("v[116:119]", "%[o1]", "1024")
               SW_LO("v[120:123]", "%[o1]", "2048") SW_LO("v[124:127]", "%[o1]", "3072")
               "s_waitcnt vmcnt(7)\n\t" SW_CK("v97", "v99", "%[e0]") "s_waitcnt vmcnt(6)\n\t" SW_CK("v101", "v103", "%[e0]")
               "s_waitcnt vmcnt(5)\n\t" SW_CK("v105", "v107", "%[e0]") "s_waitcnt vmcnt(4)\n\t" SW_CK("v109", "v111", "%[e0]")
               "s_waitcnt vmcnt(3)\n\t" SW_CK("v113", "v115", "%[e0]") "s_waitcnt vmcnt(2)\n\t" SW_CK("v117", "v119", "%[e0]")
               "s_waitcnt vmcnt(1)\n\t" SW_CK("v121", "v123", "%[e0]") "s_waitcnt vmcnt(0)\n\t" SW_CK("v125", "v127", "%[e0]") SW_END "s_nop 1"
               : "=&{v[96:99]}"(v[0]), "=&{v[100:103]}"(v[1]), "=&{v[104:107]}"(v[2]), "=&{v[108:111]}"(v[3]), "=&{v[112:115]}"(v[4]),
                 "=&{v[116:119]}"(v[5]), "=&{v[120:123]}"(v[6]), "=&{v[124:127]}"(v[7]), [n] "+s"(n)
               : [o0] "v"(o0), [o1] "v"(o1), [e0] "v"(e0), [rs] "s"(r) : "vcc", "scc", "memory");
}
__device__ __forceinline__ void sweep5(rsrc_t r, int o0, int o1, unsigned e0, v4u (&v)[5]) {
  unsigned n = SPIN_CAP;
  asm volatile("1:\n\t" SW_LO("v[108:111]", "%[o0]", "0") SW_LO("v[112:115]", "%[o0]", "1024") SW_LO("v[116:119]", "%[o0]", "2048")
               SW_LO("v[120:123]", "%[o0]", "3072") SW_LO("v[124:127]", "%[o1]", "0")
               "s_waitcnt vmcnt(4)\n\t" SW_CK("v109", "v111", "%[e0]")
               "s_waitcnt vmcnt(3)\n\t" SW_CK("v113", "v115", "%[e0]") "s_waitcnt vmcnt(2)\n\t" SW_CK("v117", "v119", "%[e0]")
               "s_waitcnt vmcnt(1)\n\t" SW_CK("v121", "v123", "%[e0]") "s_waitcnt vmcnt(0)\n\t" SW_CK("v125", "v127", "%[e0]") SW_END "s_nop 1"
               : "=&{v[108:111]}"(v[0]), "=&{v[112:115]}"(v[1]), "=&{v[116:119]}"(v[2]), "=&{v[120:123]}"(v[3]), "=&{v[124:127]}"(v[4]),
                 [n] "+s"(n)
               : [o0] "v"(o0), [o1] "v"(o1), [e0] "v"(e0), [rs] "s"(r) : "vcc", "scc", "memory");
}
__device__ __forceinline__ void glds16(const void* gsrc, unsigned lds_dst) {
  unsigned keep;
  asm volatile("s_mov_b32 %0, m0\n\ts_mov_b32 m0, %2\n\ts_nop 0\n\tglobal_load_lds_dwordx4 %1, off nt\n\ts_mov_b32 m0, %0"
               : "=&s"(keep) : "v"(gsrc), "s"(lds_dst) : "memory");
}
__device__ __forceinline__ void glds4(const void* gsrc, unsigned lds_dst) {
  unsigned keep;
  asm volatile("s_mov_b32 %0, m0\n\ts_mov_b32 m0, %2\n\ts_nop 0\n\tglobal_load_lds_dword %1, off\n\ts_mov_b32 m0, %0"
               : "=&s"(keep) : "v"(gsrc), "s"(lds_dst) : "memory");
}
__device__ __forceinline__ v4u diag(unsigned dup, const v4u& mask) { return v4u{dup & mask.x, dup & mask.y, dup & mask.z, dup & mask.w}; }
__device__ __forceinline__ v4u tr_frag(LAS unsigned char* p0, LAS unsigned char* p1) {
  const v2u a = __builtin_bit_cast(v2u, __builtin_amdgcn_ds_read_tr16_b64_v4i16((LAS s16x4*)p0));
  const v2u b = __builtin_bit_cast(v2u, __builtin_amdgcn_ds_read_tr16_b64_v4i16((LAS s16x4*)p1));
  return v4u{a.x, a.y, b.x, b.y};
}

#define FENCE() __builtin_amdgcn_sched_barrier(0)
#define FINISH() __builtin_amdgcn_endpgm()

struct Args { const u16 *x, *kc, *vc, *wq; unsigned char* ws; const u16 *wo, *wg, *wu, *wd; u16* out; };
typedef const Args __attribute__((address_space(4))) * args_t;
__device__ __forceinline__ args_t args() {
  args_t p = (args_t)__builtin_amdgcn_kernarg_segment_ptr();
  asm volatile("" : "+s"(p));
  return p;
}

__device__ __forceinline__ unsigned char* area(args_t a) {
  size_t p = ((size_t)a->ws + 0x1fffff) & ~(size_t)0x1fffff;
  asm volatile("" : "+s"(p));
  return (unsigned char*)p;
}

__global__ void __launch_bounds__(NTHR, 4) fused_block(const u16*, const u16*, const u16*, const u16*, unsigned char*,
                                                       const u16*, const u16*, const u16*, const u16*, u16*) {
  __shared__ __attribute__((aligned(16))) unsigned char lds_raw[LDS_BYTES];
  LAS unsigned char* lds = (LAS unsigned char*)lds_raw;
  volatile LAS unsigned* ctrl = (volatile LAS unsigned*)lds;
  const unsigned cb = (unsigned)(size_t)lds;
  const int tid = threadIdx.x, lane = tid & 63, wave = __builtin_amdgcn_readfirstlane(tid >> 6);
  FENCE();
  if (wave < 15)
    asm volatile("s_load_dword s98, %0, 0x0\n\ts_load_dword s99, %0, 0x40" :: "s"(__builtin_amdgcn_kernarg_segment_ptr()) : "s98", "s99");
  const int g = lane >> 4, c = lane & 15;
  const int bid = blockIdx.x, xcd = bid & 7, r = bid >> 3;
  if (wave >= W_L0) __builtin_amdgcn_s_setprio(2);
  if (wave == 15) {
    const unsigned char* pc = (const unsigned char*)__builtin_amdgcn_s_getpc();
    const int o = (r + 32 * (lane & 7)) * 128;
    glds4(pc + (o < CODE_PF ? o : 0), cb + L_SCR);
    ctrl[c] = 0u;
    LGKM0();
  }
  __builtin_amdgcn_sched_barrier(0);
  __builtin_amdgcn_s_barrier();
  __builtin_amdgcn_sched_barrier(0);
  const int h = xcd * 4 + (r & 3), e = r >> 2, b = e & 1, s = e >> 1;
  const unsigned half = (c & 1) ? 0xffff0000u : 0x0000ffffu;
  const int cj = (c & 7) >> 1;
  const int aw = (wave - W_K0) & 3;
  const size_t kvo = ((size_t)(b * H + h) * SK + (SK - WIN) + 256 * s + 64 * aw) * DH;
  const int bq = (lane >> 3) & 1;
#define XFIRST() do { lds_wait<C_X, 0>(cb, 1u); FENCE(); } while (0)
#define NEAR_OFF() (lds_wait<C_NEAR, 4>(cb, 1u), (int)(ctrl[C_NEAR] & 8192u))
#define MASK v4u{cj == 0 ? half : 0u, cj == 1 ? half : 0u, cj == 2 ? half : 0u, cj == 3 ? half : 0u}

  const int x6 = xcd >= 6, q4 = r & 3;
  const bool ffn = e == 5 || (e == 7 && !x6);
  const int ridx = q4 + (ffn ? xcd * 8 - x6 * (xcd - 6) * 4 + (e == 7) * 4 : xcd * 4 + x6 * (xcd - 6) * 4 + (e == 7) * 4);
  const bool role2 = e >= 5 && !(ffn && ridx >= 54);
#define W2_PTR(a) ((ffn ? ((wave & 1) ? a->wu : a->wg) : a->wd) + \
                   (ffn ? ((lane >> 3) + 8 * ((lane >> 2) & 1)) * F + ridx * 32 + (lane & 3) * 8 : ((wave & 1) * 32 + (lane >> 1)) * D + ridx * 16 + (lane & 1) * 8))
  if (wave >= W_L0 && role2) {
    const args_t a = args();
    const u16* p = W2_PTR(a);
    const int step = ffn ? 16 * F : 64 * D, n1 = ffn ? 2 * LG_1 : LD_1;
    const unsigned da = ffn ? GBLK : 4 * DBLK, db = ffn ? 1024 : 2 * DBLK;
    unsigned dst = cb + L_W + (wave & 1) * (ffn ? L_UP : DBLK);
    XFIRST();
#pragma nounroll
    for (int n = 0; n < n1; ++n) {
      glds16(p, dst);
      glds16(p + step, dst + db);
      p += 2 * step;
      dst += da;
    }
  }
  MEMBAR();
  if (wave == 0) {
    __builtin_amdgcn_s_setprio(3);
    const args_t a = args();
    const rsrc_t rws = mk(area(a), WS_END), rx = mk(a->x, 2 * D * 2);
    const int xa = (bq * D + (g * 8 + (lane & 7)) * 20) * 2;
    v4u f[2];
    f[0] = ld16<0>(rx, xa);
    f[1] = ld16<0>(rx, xa + 16);
    const v2u xo = __builtin_amdgcn_raw_buffer_load_b64(rx, xa + 32, 0, 0);
    const unsigned ep = __builtin_amdgcn_raw_buffer_load_b32(rws, WS_EPOCH, 0, AUX_SC1);
    __builtin_amdgcn_sched_barrier(0);
    lds_signal<C_X>(cb);
    FENCE();
    float s1 = 0.f, s2 = 0.f;
#pragma unroll
    for (int i = 0; i < 10; ++i) {
      const unsigned w = i < 8 ? f[i >> 2][i & 3] : xo[i & 1];
      s1 = dot2(w, 0x3f803f80u, s1); s2 = dot2(w, w, s2);
    }
    const float mean = sum_half8(s1) * (1.f / D);
    const float var = fmaxf(sum_half8(s2) * (1.f / D) - mean * mean, 0.f);
    *(LAS f32x2*)(lds + L_ML + 32 + bq * 8) = f32x2{rb(mean), rb(__builtin_amdgcn_sqrtf(rb(rb(var) + 1e-5f)))};
    ctrl[C_TAG] = ep + 1u;
    ctrl[C_T0] = (unsigned)__builtin_amdgcn_s_memrealtime();
    lds_signal<C_LN>(cb);
    FENCE();
  }
  MEMBAR();
  if (wave >= 1 && wave < 5 && e < 5) {
    const int sg = wave - 1;
    __builtin_amdgcn_s_setprio(3);
    const args_t a = args();
    const int row = 128 * e + 32 * sg + 8 * g;
    asm volatile("" :: "s"(a->wq), "s"(a->x));
    XFIRST();
    const v4u xo = ld16<0>(mk(a->x, 2 * D * 2), (bq * D + row) * 2);
    v4u f[8];
#pragma unroll
    for (int i = 0; i < 8; ++i) f[i] = ld16<AUX_NT>(mk(a->wq, D * H * DH * 2), row * 8192 + h * 256 + c * 16, i * 8192);
    __builtin_amdgcn_sched_barrier(0);
    const rsrc_t rws = mk(area(a), WS_END);
    const v4u mask = MASK;
    lds_wait<C_LN, 1>(cb, 1u);
    const f32x2 st = *(LAS f32x2*)(lds + L_ML + 32 + bq * 8);
    const unsigned tag = ctrl[C_TAG];
    const float rden = __builtin_amdgcn_rcpf(st.y);
    f32x4 acc = f32x4{0.f, 0.f, 0.f, 0.f};
#pragma unroll
    for (int i = 0; i < 8; ++i) {
      const float xn = fdiv(rb(((i & 1) ? bhi(xo[i >> 1]) : blo(xo[i >> 1])) - st.x), st.y, rden);
      acc = mfma(diag(pk(xn, xn), mask), f[i], acc);
    }
    *(LAS f32x4*)(lds + L_QP + sg * 1024 + lane * 16) = acc;
    const unsigned tk = lds_arrive<C_QP>(cb);
    f32x4 t[4];
#pragma unroll
    for (int w = 0; w < 4; ++w) t[w] = *(LAS f32x4*)(lds + L_QP + w * 1024 + lane * 16);
    f32x4 u = (t[0] + t[1]) + (t[2] + t[3]);
    PIN(u);
    if (ticket(tk) == 3u) {
      const int qo = WS_QP + ((h * 2 + (g >> 1)) * 8 + e) * 1024 + (lane & 31) * 16;
      st16(rws, qo, v4u{__float_as_uint(u[0]), tag, __float_as_uint(u[1]), tag});
      st16(rws, qo + 512, v4u{__float_as_uint(u[2]), tag, __float_as_uint(u[3]), tag});
    }
    __builtin_amdgcn_s_setprio(0);
    FENCE();
  }
  MEMBAR();
  if (wave == 5) {
    const args_t a = args();
    unsigned char* ws = area(a);
    const rsrc_t rws = mk(ws, WS_END);
    const int so = WS_SC + (bid >> 5) * 32768 + (bid & 31) * 128;
    __builtin_amdgcn_raw_buffer_store_b32(0u, rws, lane < 8 ? so + lane * 4096 : OOB, 0, AUX_SC1);
    asm volatile("s_waitcnt vmcnt(0)" ::: "memory");
    const unsigned char* sp = ws + so;
    unsigned best[2] = {~0u, ~0u};
#pragma unroll
    for (int k = 0; k < 8; ++k) {
      unsigned long long t0, t1;
      unsigned d;
      asm volatile("s_memtime %0\n\ts_waitcnt lgkmcnt(0)\n\ts_load_dword %2, %3, %4\n\ts_waitcnt lgkmcnt(0)\n\ts_memtime %1\n\ts_waitcnt lgkmcnt(0)"
                   : "=&s"(t0), "=&s"(t1), "=&s"(d) : "s"(sp), "i"(k * 4096) : "memory");
      const unsigned dt = (unsigned)(t1 - t0);
      best[(k >> 1) & 1] = dt < best[(k >> 1) & 1] ? dt : best[(k >> 1) & 1];
    }
    ctrl[C_NEAR] = best[1] < best[0] ? 8193u : 1u;
  }
  MEMBAR();
  if (wave == W_V0) {
    const args_t a = args();
    const size_t kv = ((size_t)(b * H + h) * SK + (SK - WIN) + 256 * s) * DH;
    const unsigned char* p = (const unsigned char*)(lane == 0 ? a->kc + kv : lane == 1 ? a->vc + kv : lane == 2 ? a->out : a->wo + (size_t)(h * DH + (lane & 3) * 42) * D);
    glds4(p, cb + L_SCR + 256);
  }
  MEMBAR();
  if (wave >= W_K0 && wave < W_V0) {
    const args_t a = args();
    const rsrc_t rkv = mk(a->kc + kvo, 16384), rws = mk(area(a), WS_END);
    const int lk = (c >> 2) * 256 + g * 64 + (c & 3) * 16;
    lds_wait<C_LN, 1>(cb, 1u);
    unsigned tn = (unsigned)__builtin_amdgcn_readfirstlane(ctrl[C_T0]) + (e >= 5 ? PACE0F : PACE0);
    const unsigned pa = e >= 5 ? PF : PA;
    v4u f[16];
#pragma unroll
    for (int i = 0; i < 16; ++i) {
      tick(tn, pa);
      if (i == 14) { ctrl[C_T1 + aw] = tn + pa; lds_signal<C_KV>(cb); }
      f[i] = ld16<AUX_NT>(rkv, lk + (i & 3) * 1024, (i >> 2) * 4096);
      if (i >= DEPTH && i < 15) { __builtin_amdgcn_sched_barrier(0); PIN(f[i - DEPTH]); }
    }
    FENCE();
    lds_wait<C_QRDY, 2>(cb, 1u);
    __builtin_amdgcn_s_setprio(3);
    const v4u qf = *(LAS v4u*)(lds + L_Q + (4 * g + (c & 3)) * 16);
    const unsigned tag = ctrl[C_TAG];
    v4u qm[4];
#pragma unroll
    for (int j = 0; j < 4; ++j) qm[j] = (c >> 2) == j ? qf : v4u{0u, 0u, 0u, 0u};
    float y[4];
#pragma unroll
    for (int k = 0; k < 4; ++k) {
      f32x4 d = f32x4{0.f, 0.f, 0.f, 0.f};
#pragma unroll
      for (int j = 0; j < 4; ++j) d = mfma(f[4 * k + j], qm[j], d);
      y[k] = (dpp<0x00>(d[0]) + dpp<0x55>(d[1])) + (dpp<0xAA>(d[2]) + dpp<0xFF>(d[3]));
    }
    float sv = (c & 2) ? ((c & 1) ? y[3] : y[2]) : ((c & 1) ? y[1] : y[0]);
    sv = rb(rb(sv) * 0.08838834764831845f);
    float m = sv;
    m = fmaxf(m, dpp<XOR1>(m)); m = fmaxf(m, dpp<XOR2>(m)); m = fmaxf(m, dpp<ROR4>(m)); m = fmaxf(m, dpp<ROR8>(m));
    m = max32(max16(m));
    *(LAS float*)(lds + L_ML + aw * 4) = m;
    lds_signal<C_M>(cb);
    lds_wait<C_M, 1>(cb, 4u);
    const f32x4 m4 = *(LAS f32x4*)(lds + L_ML);
    m = fmaxf(fmaxf(m4[0], m4[1]), fmaxf(m4[2], m4[3]));
    const float p = exp_e(sv - m);
    *(LAS unsigned*)(lds + L_P + aw * 256 + (g * 16 + 4 * (c & 3) + (c >> 2)) * 4) = pk(p, p);
    float l = p;
    l += dpp<XOR1>(l); l += dpp<XOR2>(l); l += dpp<HMIR>(l); l += dpp<ROR8>(l);
    l = sum32(sum16(l));
    *(LAS float*)(lds + L_ML + 16 + aw * 4) = l;
    const unsigned tk = lds_arrive<C_P>(cb);
    const f32x4 l4 = *(LAS f32x4*)(lds + L_ML + 16);
    float lt = (l4[0] + l4[1]) + (l4[2] + l4[3]);
    PIN(lt);
    if (ticket(tk) == 3u) st16(rws, lane == 0 ? WS_ML + h * 128 + e * 16 : OOB, v4u{__float_as_uint(m), tag, __float_as_uint(lt), tag});
    __builtin_amdgcn_s_setprio(0);
    FENCE();
  }
  MEMBAR();
  if (wave >= W_V0 && wave < W_L0) {
    const args_t a = args();
    const rsrc_t rkv = mk(a->vc + kvo, 16384), rws = mk(area(a), WS_END);
    const v4u mask = MASK;
    int oaddr = L_O + aw * 512 + c * 32 + (g & 1) * 16;
    int po = WS_OA + h * 16384 + g * 2048 + b * 1024 + s * 256 + c * 16, po1 = po + 8192;
    PIN(oaddr); PIN(po); PIN(po1);
    lds_wait<C_KV, 2>(cb, 4u);
    unsigned tn = (unsigned)__builtin_amdgcn_readfirstlane(ctrl[C_T1 + aw]);
    const unsigned ps = e >= 5 ? PF : PA, pt = e >= 5 ? PF : PB;
    v4u f[16];
#pragma unroll
    for (int i = 0; i < 16; ++i) {
      tick(tn, 16u + i < PN ? ps : pt);
      f[i] = ld16<AUX_NT>(rkv, lane * 16 + (i & 3) * 1024, (i >> 2) * 4096);
      if (i == 14) { __builtin_amdgcn_sched_barrier(0); lds_signal<C_KV>(cb); }
      if (i >= DEPTH && i < 15) { __builtin_amdgcn_sched_barrier(0); PIN(f[i - DEPTH]); }
    }
    FENCE();
    lds_wait<C_P, 1>(cb, 4u);
    __builtin_amdgcn_s_setprio(3);
    const unsigned tag = ctrl[C_TAG];
    v4u pd[4];
#pragma unroll
    for (int i = 0; i < 4; ++i) pd[i] = *(LAS v4u*)(lds + L_P + aw * 256 + g * 64 + i * 16);
    FENCE();
    f32x4 o = f32x4{0.f, 0.f, 0.f, 0.f};
    v4u pa = diag(pd[0][0], mask);
#pragma unroll
    for (int i = 0; i < 16; ++i) {
      v4u nx = pa;
      if (i < 15) nx = diag(pd[(i + 1) >> 2][(i + 1) & 3], mask);
      PIN(nx);
      __builtin_amdgcn_sched_barrier(0);
      o = mfma(pa, f[i], o);
      pa = nx;
    }
    *(LAS f32x4*)(lds + oaddr) = o;
    const unsigned tk = lds_arrive<C_O>(cb);
    f32x2 t[4];
#pragma unroll
    for (int w = 0; w < 4; ++w) t[w] = *(LAS f32x2*)(lds + L_O + w * 512 + lane * 8);
    f32x2 u = (t[0] + t[1]) + (t[2] + t[3]);
    PIN(u);
    if (ticket(tk) == 3u) {
      st16(rws, po, v4u{__float_as_uint(u.x), tag, __float_as_uint(u.y), tag});
      st16(rws, po1, v4u{__float_as_uint(u.x), tag, __float_as_uint(u.y), tag});
    }
    FENCE();
    FINISH();
  }
  if (wave == 0) {
    const args_t a = args();
    const rsrc_t rws = mk(area(a), WS_END), rx = mk(a->x, 2 * D * 2);
    const unsigned tag = ctrl[C_TAG];
    v4u v[8], vq[5];
    int off[5];
    unsigned et[5];
#pragma unroll
    for (int i = 0; i < 5; ++i) { off[i] = WS_QP + (h * 2 + b) * 8192 + lane * 16 + i * 1024; et[i] = tag; }
    if (e < 5) lds_wait<C_QP, 1>(cb, 4u);
    __builtin_amdgcn_s_sleep(36);
    wait_until((unsigned)__builtin_amdgcn_readfirstlane(ctrl[C_T0]) + QT);
    FENCE();
    sweep<5, 16>(rws, off, et, vq);
    float q0 = 0.f, q1 = 0.f;
#pragma unroll
    for (int i = 0; i < 5; ++i) { q0 += __uint_as_float(vq[i].x); q1 += __uint_as_float(vq[i].z); }
    *(LAS unsigned*)(lds + L_Q + (4 * c + 2 * (g & 1) + (g >> 1)) * 4) = pk(q0, q1);
    lds_signal<C_QRDY>(cb);
    FENCE();
    if (r < 5) {
      const int rd = xcd * 5 + r;
      const unsigned x0 = __builtin_amdgcn_raw_buffer_load_b16(rx, (bq * D + rd * 16 + (lane & 7)) * 2, 0, 0);
      const unsigned x1 = __builtin_amdgcn_raw_buffer_load_b16(rx, (bq * D + rd * 16 + 8 + (lane & 7)) * 2, 0, 0);
      int o0 = WS_PB + rd * 16384 + NEAR_OFF() + lane * 16, o1 = o0 + 4096;
      int hoff = g < 2 ? WS_HB + g * 8192 + rd * 128 + c * 8 : OOB;
      float xa = blo(x0), xb = blo(x1);
      PIN(o0); PIN(o1); PIN(hoff); PIN(xa); PIN(xb);
      __builtin_amdgcn_s_setprio(0);
      lds_wait<C_WO, 4>(cb, 4u);
      __builtin_amdgcn_s_setprio(3);
      sweep8(rws, o0, o1, tag, v);
      FENCE();
      f32x2 t[8];
#pragma unroll
      for (int i = 0; i < 8; ++i) t[i] = f32x2{__uint_as_float(v[i].x), __uint_as_float(v[i].z)};
      const f32x2 u = ((t[0] + t[1]) + (t[2] + t[3])) + ((t[4] + t[5]) + (t[6] + t[7]));
      const unsigned ab = pk(sum32(sum16(u.x)), sum32(sum16(u.y)));
      __builtin_amdgcn_raw_buffer_store_b64(v2u{pk(xa + blo(ab), xb + bhi(ab)), tag}, rws, hoff, 0, AUX_SC1);
      FENCE();
    }
    FINISH();
  }
  if (e < 5) {
    if (wave < 5) {
      const int w = wave - 1;
      const args_t a = args();
      const rsrc_t rws = mk(area(a), WS_END);
      const rsrc_t ro = mk(a->wo + (size_t)(h * DH + 32 * w) * D, 32 * D * 2);
      v4u sel0, sel1;
#pragma unroll
      for (int j = 0; j < 4; ++j) {
        sel0[j] = cj == j ? ((c & 1) ? 0x01000c0cu : 0x0c0c0100u) : 0x0c0c0c0cu;
        sel1[j] = cj == j ? ((c & 1) ? 0x03020c0cu : 0x0c0c0302u) : 0x0c0c0c0cu;
      }
      const unsigned tag = ctrl[C_TAG];
      int mo[1], off[2];
      const unsigned e1[1] = {tag};
      mo[0] = WS_ML + h * 128 + (c >> 2) * 32 + (c & 1) * 16;
      off[0] = WS_OA + h * 16384 + NEAR_OFF() + w * 2048 + (c >> 2) * 256 + g * 64 + (c & 3) * 16;
      off[1] = off[0] + 1024;
      int pbo = WS_PB + (e * 8 + 2 * g) * 16384 + h * 256 + c * 16;
      int wa = L_QP + w * 1024 + lane * 16, ra = L_QP + lane * 16;
      PIN(sel0); PIN(sel1); PIN(mo[0]); PIN(off[0]); PIN(off[1]); PIN(pbo); PIN(wa); PIN(ra);
      lds_wait<C_KV, 2>(cb, 8u);
      const unsigned tw = (unsigned)__builtin_amdgcn_s_memrealtime() + WOFF;
      v4u wv[8], vm[1];
#pragma unroll
      for (int i = 0; i < 8; ++i) {
        wait_until(tw + i * WPACE);
        wv[i] = ld16<AUX_NT>(ro, g * 8 * 1280 + e * 256 + c * 16, i * 1280);
        if (i == 4) vm[0] = ld16<AUX_SC1>(rws, mo[0]);
      }
      FENCE();
      if (__builtin_amdgcn_ballot_w64(((vm[0].y ^ tag) | (vm[0].w ^ tag)) != 0u) != 0ull) sweep<1>(rws, mo, e1, vm);
      const float mp = __uint_as_float(vm[0].x);
      float mx = fmaxf(mp, dpp<ROR8>(mp)); mx = fmaxf(mx, dpp<ROR4>(mx));
      const float wt = exp_e(mp - mx);
      float lt = wt * __uint_as_float(vm[0].z);
      lt += dpp<ROR8>(lt); lt += dpp<ROR4>(lt);
      const float cf = wt * __builtin_amdgcn_rcpf(lt);
      float c0 = dpp<0x00>(cf), c1 = dpp<0x55>(cf);
      PIN(c0); PIN(c1);
      lds_wait<C_O, 1>(cb, 4u);
      __builtin_amdgcn_s_setprio(2);
      FENCE();
      v4u v[2];
      sweep2<false>(rws, off[0], off[1], tag, tag, v[0], v[1]);
      FENCE();
      const float p0 = c0 * __uint_as_float(v[0].x), p1 = c0 * __uint_as_float(v[0].z), q0 = c1 * __uint_as_float(v[1].x), q1 = c1 * __uint_as_float(v[1].z);
      const float r0 = p0 + dpp<ROR8>(p0), r1 = p1 + dpp<ROR8>(p1), t0 = q0 + dpp<ROR8>(q0), t1 = q1 + dpp<ROR8>(q1);
      const float u0 = r0 + dpp<ROR4>(r0), u1 = r1 + dpp<ROR4>(r1), w0 = t0 + dpp<ROR4>(t0), w1 = t1 + dpp<ROR4>(t1);
      const unsigned op = c >= 8 ? pk(w0, w1) : pk(u0, u1);
      const unsigned bc[4] = {dppu<0x00>(op), dppu<0x55>(op), dppu<0xAA>(op), dppu<0xFF>(op)};
      f32x4 acc = f32x4{0.f, 0.f, 0.f, 0.f};
      v4u da[8];
#pragma unroll
      for (int i = 0; i < 8; ++i) {
        const v4u& sl = (i & 1) ? sel1 : sel0;
        const unsigned src = bc[i >> 1];
        da[i] = v4u{__builtin_amdgcn_perm(0u, src, sl.x), __builtin_amdgcn_perm(0u, src, sl.y), __builtin_amdgcn_perm(0u, src, sl.z), __builtin_amdgcn_perm(0u, src, sl.w)};
        acc = mfma(wv[i], da[i], acc);
      }
#pragma unroll
      for (int i = 0; i < 8; ++i) asm volatile("" :: "v"(da[i]), "v"(wv[i]));
      *(LAS f32x4*)(lds + wa) = acc;
      const unsigned tk = lds_arrive<C_WO>(cb);
      f32x4 t[4];
#pragma unroll
      for (int k = 0; k < 4; ++k) t[k] = *(LAS f32x4*)(lds + ra + k * 1024);
      f32x4 u = (t[0] + t[1]) + (t[2] + t[3]);
      PIN(u);
      if (ticket(tk) == 3u) {
        const v4u s0 = v4u{__float_as_uint(u[0]), tag, __float_as_uint(u[1]), tag}, s1 = v4u{__float_as_uint(u[2]), tag, __float_as_uint(u[3]), tag};
        __builtin_amdgcn_raw_buffer_store_b128(s0, rws, pbo, 0, AUX_SC1);
        __builtin_amdgcn_raw_buffer_store_b128(s0, rws, pbo, 8192, AUX_SC1);
        __builtin_amdgcn_raw_buffer_store_b128(s1, rws, pbo, 16384, AUX_SC1);
        __builtin_amdgcn_raw_buffer_store_b128(s1, rws, pbo, 24576, AUX_SC1);
      }
      FENCE();
    }
    FINISH();
  }

  if (!role2) FINISH();
  if (wave >= W_L0) {
    const int ld = wave & 1;
    const args_t a = args();
    const unsigned lw = cb + L_W;
    const u16* p = W2_PTR(a);
    PIN(p);
    __builtin_amdgcn_s_setprio(2);
#define LOADER_START lds_wait<C_KV, 2>(cb, 8u); lds_wait<C_P, 2>(cb, 4u); const unsigned te = (unsigned)__builtin_amdgcn_s_memrealtime() + (ffn ? LOFF : LOFD); FENCE()
    if (ffn) {
      LOADER_START;
#pragma nounroll
      for (int i4 = LG_1; i4 < 10; ++i4) {
        if (i4 < LG_1 + 3) wait_until(te + (i4 - LG_1) * 4 * LG_P);
        const u16* q = p + (size_t)i4 * 64 * F;
#pragma unroll
        for (int j = 0; j < 4; ++j) {
          glds16(q + (size_t)j * 16 * F, lw + ld * L_UP + (2 * i4 + (j >> 1)) * GBLK + (j & 1) * 1024);
          asm volatile("s_waitcnt vmcnt(%0)" :: "i"(LG_D) : "memory");
        }
        if (i4 == 6) lds_signal<C_W0>(cb + ld * 4);
      }
      asm volatile("s_waitcnt vmcnt(8)" ::: "memory");
      lds_signal<C_W0>(cb + ld * 4);
    } else {
      LOADER_START;
#pragma nounroll
      for (int i = 2 * LD_1; i < 27; ++i) {
        if (i == 2 * LD_1) wait_until(te);
        if (i == 2 * LD_1 + 3) wait_until(te + 3 * LD_P);
        glds16(p + (size_t)i * 64 * D, lw + (2 * i + ld) * DBLK);
        asm volatile("s_waitcnt vmcnt(%0)" :: "i"(LD_D) : "memory");
      }
    }
    FENCE();
    asm volatile("s_waitcnt vmcnt(0)" ::: "memory");
    lds_signal<C_W0>(cb + (ffn ? ld * 4 : 0));
    FENCE();
    FINISH();
  }

  const int cw = wave - 1;
  const args_t ar = args();
  const rsrc_t rws = mk(area(ar), WS_END);
  lds_wait<C_LN, 4>(cb, 1u);
  const unsigned tag = ctrl[C_TAG];
  const bool hi = g >= 2;
  const int near = NEAR_OFF();
#define PICK(A) (((hi ? A[4][0] : A[0][0]) + (hi ? A[5][1] : A[1][1])) + ((hi ? A[6][2] : A[2][2]) + (hi ? A[7][3] : A[3][3])))
  if (ffn) {
    if (cw >= 8) FINISH();
    const int kp = cw >> 1, mat = cw & 1;
    int ao = WS_AC + (ridx >> 5) * 16384 + (lane >> 5) * 8192 + (ridx & 31) * 256 + (lane & 31) * 8;
    int ry = L_DN + (lane & 31) * 8, rc = L_CS + (g & 1) * 64 + (lane & 7) * 8, rs = L_ST + bq * 8;
    PIN(ao); PIN(ry); PIN(rc); PIN(rs);
    if (kp == 3) {
      LAS unsigned char* wb = lds + L_W + mat * L_UP + g * GBLK + (c >> 2) * 64 + (c & 3) * 8;
      const v4u ones = v4u{0x3f803f80u, 0x3f803f80u, 0x3f803f80u, 0x3f803f80u};
      f32x4 cs0 = f32x4{0.f, 0.f, 0.f, 0.f}, cs1 = cs0;
#pragma nounroll
      for (int k = 0; k < 5; ++k) {
        lds_wait<C_W0, 2>(cb + mat * 4, (unsigned)((k >> 1) + 1));
#pragma unroll
        for (int u = 0; u < 4; ++u) {
          cs0 = mfma(ones, tr_frag(wb + u * 512, wb + u * 512 + 256), cs0);
          cs1 = mfma(ones, tr_frag(wb + u * 512 + 32, wb + u * 512 + 288), cs1);
        }
        wb += 4 * GBLK;
      }
      *(LAS float*)(lds + L_CS + mat * 128 + (g >> 1) * 64 + c * 4) = hi ? cs1[0] : cs0[0];
    }
    MEMBAR();
    if (cw == 7) {
      int o0 = WS_HB + near + lane * 16, o1 = o0 + 4096, sa = L_ST + ((c >> 2) & 1) * 8;
      PIN(o0); PIN(o1); PIN(sa);
      lds_wait<C_W0, 1>(cb, 3u);
      __builtin_amdgcn_s_setprio(3);
      v4u v[5];
      sweep5(rws, o0, o1, tag, v);
      FENCE();
      float s1 = 0.f, s2 = 0.f, s3 = 0.f, s4 = 0.f;
#pragma unroll
      for (int i = 0; i < 5; ++i) {
        s1 = dot2(v[i].x, 0x3f803f80u, s1); s2 = dot2(v[i].x, v[i].x, s2);
        s3 = dot2(v[i].z, 0x3f803f80u, s3); s4 = dot2(v[i].z, v[i].z, s4);
      }
      s1 += s3; s2 += s4;
      s1 += dpp<XOR1>(s1); s2 += dpp<XOR1>(s2); s1 += dpp<XOR2>(s1); s2 += dpp<XOR2>(s2); s1 += dpp<ROR8>(s1); s2 += dpp<ROR8>(s2);
      const float mean = sum32(sum16(s1)) * (1.f / D);
      const float var = fmaxf(sum32(sum16(s2)) * (1.f / D) - mean * mean, 0.f);
      *(LAS f32x2*)(lds + sa) = f32x2{rb(mean), __builtin_amdgcn_rcpf(rb(__builtin_amdgcn_sqrtf(rb(rb(var) + 1e-5f))))};
    }
    MEMBAR();
    if (kp < 3) {
      LAS unsigned char* wb = lds + L_W + mat * L_UP + (kp * 8 + g) * GBLK + (c >> 2) * 64 + (c & 3) * 8;
      LAS unsigned char* wb1 = wb + (kp == 2 ? 0 : 4 * GBLK);
      int g0 = WS_HB + near + kp * 2048 + lane * 16, g1 = kp == 2 ? OOB : g0 + 1024, wy = L_DN + cw * 256 + lane * 4;
      unsigned e1 = kp == 2 ? 0u : tag;
      PIN(g0); PIN(g1); PIN(e1); PIN(wy);
      lds_wait<C_W0, 1>(cb + mat * 4, (unsigned)(kp + 1));
      v4u bw[2][8];
#pragma unroll
      for (int t = 0; t < 2; ++t)
#pragma unroll
        for (int u = 0; u < 8; ++u) bw[t][u] = tr_frag(wb + u * 256 + t * 32, wb1 + u * 256 + t * 32);
      lds_wait<C_W0, 1>(cb, 3u);
      lds_wait<C_W1, 1>(cb, 3u);
#pragma unroll
      for (int u = 0; u < 8; ++u) { PIN(bw[0][u]); PIN(bw[1][u]); }
      FENCE();
      v4u af, v1;
      sweep2<true>(rws, g0, g1, tag, e1, af, v1);
      FENCE();
      f32x4 a0[8], a1[8];
#pragma unroll
      for (int u = 0; u < 8; ++u) a0[u] = mfma(af, bw[0][u], f32x4{0.f, 0.f, 0.f, 0.f});
#pragma unroll
      for (int u = 0; u < 8; ++u) a1[u] = mfma(af, bw[1][u], f32x4{0.f, 0.f, 0.f, 0.f});
      __builtin_amdgcn_sched_barrier(0);
      const auto sw = __builtin_amdgcn_permlane32_swap(__float_as_uint(PICK(a0)), __float_as_uint(PICK(a1)), false, false);
      *(LAS float*)(lds + wy) = __uint_as_float(sw[0]) + __uint_as_float(sw[1]);
#pragma unroll
      for (int u = 0; u < 8; ++u) asm volatile("" :: "v"(a0[u]), "v"(a1[u]));
    }
    const unsigned tk = lds_arrive<C_DN>(cb);
    f32x2 y[6];
#pragma unroll
    for (int k = 0; k < 6; ++k) y[k] = *(LAS f32x2*)(lds + ry + k * 256);
    f32x2 csg = *(LAS f32x2*)(lds + rc), csu = *(LAS f32x2*)(lds + rc + 128), st = *(LAS f32x2*)(lds + rs);
    __builtin_amdgcn_sched_barrier(0);
    const f32x2 gg = (((y[0] + y[2]) + y[4]) - st.x * csg) * st.y, uu = (((y[1] + y[3]) + y[5]) - st.x * csu) * st.y;
    unsigned gp = pk(gg.x, gg.y), up = pk(uu.x, uu.y);
    PIN(gp); PIN(up);
    if (ticket(tk) == 7u) {
      const float g0 = blo(gp), g1 = bhi(gp);
      const unsigned sp = pk(g0 * __builtin_amdgcn_rcpf(1.f + exp_e(-g0)), g1 * __builtin_amdgcn_rcpf(1.f + exp_e(-g1)));
      const v2u gr = v2u{pk(blo(sp) * blo(up), bhi(sp) * bhi(up)), tag};
      __builtin_amdgcn_raw_buffer_store_b64(gr, rws, ao, 0, AUX_SC1);
      FENCE();
    }
    FINISH();
  }
  if (cw >= 8) FINISH();
  const rsrc_t rout = mk(ar->out, 2 * D * 2);
  int oo = lane < 16 ? (bq * D + ridx * 16 + (lane & 7) * 2) * 2 : OOB, ry = L_DN + c * 8;
  PIN(oo); PIN(ry);
  if (cw == 7) {
    lds_wait<C_W0, 1>(cb, 2u);
    v4u v[1];
    const int off[1] = {WS_HB + near + ridx * 128 + (lane & 7) * 16};
    const unsigned et[1] = {tag};
    sweep<1>(rws, off, et, v);
    __builtin_amdgcn_raw_buffer_store_b32(tag, rws, (ridx == 0 && lane == 0) ? WS_EPOCH : OOB, 0, AUX_SC1);
    *(LAS f32x2*)(lds + L_DN + 7 * 128 + ((lane >> 2) & 1) * 64 + (lane & 3) * 8) = f32x2{blo(v[0].x), blo(v[0].z)};
    *(LAS f32x2*)(lds + L_DN + 7 * 128 + ((lane >> 2) & 1) * 64 + (lane & 3) * 8 + 32) = f32x2{bhi(v[0].x), bhi(v[0].z)};
    FENCE();
  }
  MEMBAR();
  if (cw < 7) {
    const bool t1 = cw * 8 + 4 + g < 54;
    LAS unsigned char* wb = lds + L_W + (cw * 8 + g) * DBLK + (c >> 2) * 32 + (c & 3) * 8;
    LAS unsigned char* wb1 = wb + (t1 ? 4 * DBLK : 0);
    int g0 = WS_AC + (cw >> 2) * 16384 + near + (cw & 3) * 2048 + lane * 16, g1 = t1 ? g0 + 1024 : OOB, wy = L_DN + cw * 128 + (lane & 31) * 4;
    unsigned e1 = t1 ? tag : 0u;
    PIN(g0); PIN(g1); PIN(e1); PIN(wy); PIN(wb); PIN(wb1);
    lds_wait<C_W0, 1>(cb, 2u);
    v4u bw[8];
#pragma unroll
    for (int u = 0; u < 8; ++u) bw[u] = tr_frag(wb + u * 128, wb1 + u * 128);
#pragma unroll
    for (int u = 0; u < 8; ++u) PIN(bw[u]);
    FENCE();
    v4u af, v1;
    sweep2<true>(rws, g0, g1, tag, e1, af, v1);
    FENCE();
    f32x4 acc[8];
#pragma unroll
    for (int u = 0; u < 8; ++u) acc[u] = mfma(af, bw[u], f32x4{0.f, 0.f, 0.f, 0.f});
    __builtin_amdgcn_sched_barrier(0);
    *(LAS float*)(lds + wy) = sum32(PICK(acc));
#pragma unroll
    for (int u = 0; u < 8; ++u) asm volatile("" :: "v"(acc[u]));
  }
  const unsigned tk = lds_arrive<C_DN>(cb);
  f32x2 t[8];
#pragma unroll
  for (int w = 0; w < 8; ++w) t[w] = *(LAS f32x2*)(lds + ry + w * 128);
  __builtin_amdgcn_sched_barrier(0);
  const f32x2 a = ((t[0] + t[1]) + (t[2] + t[3])) + ((t[4] + t[5]) + t[6]);
  const unsigned ap = pk(a.x, a.y);
  unsigned res = pk(t[7].x + blo(ap), t[7].y + bhi(ap));
  PIN(res);
  if (ticket(tk) == 7u) {
    __builtin_amdgcn_raw_buffer_store_b32(res, rout, oo, 0, AUX_SC1);
    FENCE();
  }
  FINISH();
}
}

extern "C" void kernel_launch(void* const* d_in, const int* in_sizes, int n_in, void* d_out, int out_size, void* d_ws, size_t ws_size,
                              hipStream_t stream) {
  fused_block<<<NWG, NTHR, 0, stream>>>((const u16*)d_in[0], (const u16*)d_in[1], (const u16*)d_in[2], (const u16*)d_in[3], (unsigned char*)d_ws,
                                        (const u16*)d_in[4], (const u16*)d_in[6], (const u16*)d_in[7], (const u16*)d_in[8], (u16*)d_out);
}
```
